```python
import torch

def _softmax(x, axis=-1):
    return torch.softmax(x, dim=axis)
def _gelu(x):
    return torch.nn.functional.gelu(x, approximate="tanh")
def _silu(x):
    return torch.nn.functional.silu(x)
def _sigmoid(x):
    return torch.sigmoid(x)
def _layernorm(x, axis=-1, eps=1e-5):
    m = x.mean(dim=axis, keepdim=True)
    v = x.var(dim=axis, keepdim=True, unbiased=False)
    return (x - m) / torch.sqrt(v + eps)
def _scatter_add(tgt, idx, src, axis=0):
    return tgt.clone().index_add_(axis, idx, src)
def _segment_sum(src, seg, n):
    out = torch.zeros((n,) + src.shape[1:], dtype=src.dtype)
    return out.index_add_(0, seg, src)
def _pad(x, pw):
    flat = tuple(v for pair in reversed(pw) for v in pair)
    return torch.nn.functional.pad(x, flat)

kernel_name = 'sequential_preLN__sliding_block_cached__glu_gated_bilinear__decode'

def setup_inputs():

    _w = (lambda a: (lambda t: t.to(torch.bfloat16) if t.is_floating_point() else t)(torch.as_tensor(a)))
    def _A_setup():
        B, Sq, D = 4, 1, 512
        Hq, Hkv, Sk, Dh = 32, 32, 4096, 128
        assert Hq % Hkv == 0, (Hq, Hkv)
        return {
            "x": _w((lambda *s: torch.randn(*s, dtype=torch.bfloat16) * 0.05)(B, Sq, D)),
            "k_cache": _w((lambda *s: torch.randn(*s, dtype=torch.bfloat16) * 1.6)(B, Hkv, Sk, Dh)),
            "v_cache": _w((lambda *s: torch.randn(*s, dtype=torch.bfloat16) * 0.8)(B, Hkv, Sk, Dh)),
            "wq": _w((lambda *s: torch.randn(*s, dtype=torch.bfloat16) * 1.6)(D, Hq * Dh) * (D ** -0.5)),
            "wo": _w((lambda *s: torch.randn(*s, dtype=torch.bfloat16) * 0.8)(Hq * Dh, D) * ((Hq * Dh) ** -0.5)),
        }
    _A_in = _A_setup()

    def _B_setup():
        B, S, D, F = 4, 1, 512, 1376
        return {
            "x": _w((lambda *s: torch.randn(*s, dtype=torch.bfloat16) * 0.05)(B, S, D)),
            "w_gate": _w((lambda *s: torch.randn(*s, dtype=torch.bfloat16) * 0.4)(D, F) * (D ** -0.5)),
            "w_up": _w((lambda *s: torch.randn(*s, dtype=torch.bfloat16) * 0.4)(D, F) * (D ** -0.5)),
            "w_down": _w((lambda *s: torch.randn(*s, dtype=torch.bfloat16) * 0.4)(F, D) * (F ** -0.5)),
        }
    _B_in = _B_setup()

    return {
        "A_x": _A_in["x"],
        "A_k_cache": _A_in["k_cache"],
        "A_v_cache": _A_in["v_cache"],
        "A_wq": _A_in["wq"],
        "A_wo": _A_in["wo"],
        "B_x": _B_in["x"],
        "B_w_gate": _B_in["w_gate"],
        "B_w_up": _B_in["w_up"],
        "B_w_down": _B_in["w_down"],
    }

def reference(A_x, A_k_cache, A_v_cache, A_wq, A_wo, B_x, B_w_gate, B_w_up, B_w_down):
    def _A_ref(x, k_cache, v_cache, wq, wo):
        B, Sq, D = x.shape
        Hq, Dh = 32, 128
        Hkv, Sk = k_cache.shape[1], k_cache.shape[2]
        W = min(4096, max(256, Sk // 4))
        rep = Hq // Hkv
        q = (lambda x, *d: x.permute(*d))((x @ wq).reshape(B, Sq, Hq, Dh), 0, 2, 1, 3)
        k = (lambda x, n, axis: torch.repeat_interleave(x, n, dim=axis))(k_cache[:, :, Sk - W:, :], rep, axis=1)
        v = (lambda x, n, axis: torch.repeat_interleave(x, n, dim=axis))(v_cache[:, :, Sk - W:, :], rep, axis=1)
        scores = torch.einsum("bhqd,bhkd->bhqk", q, k) * (Dh ** -0.5)
        p = _softmax(scores, axis=-1)
        o = torch.einsum("bhqk,bhkd->bhqd", p, v)
        return (lambda x, *d: x.permute(*d))(o, 0, 2, 1, 3).reshape(B, Sq, Hq * Dh) @ wo

    def _B_ref(x, w_gate, w_up, w_down):
        g = torch.einsum("bsd,df->bsf", x, w_gate)
        u = torch.einsum("bsd,df->bsf", x, w_up)
        h = g * u
        return torch.einsum("bsf,fd->bsd", h, w_down)

    x = A_x
    h = x + _A_ref(_layernorm(x, axis=-1), A_k_cache, A_v_cache, A_wq, A_wo)
    h2 = h + _B_ref(_layernorm(h, axis=-1), B_w_gate, B_w_up, B_w_down)
    return h2
```

```cpp
#include <hip/hip_runtime.h>
#include <hip/hip_bf16.h>
#include <stdint.h>

typedef unsigned short u16;
typedef unsigned int u32;
typedef __attribute__((ext_vector_type(4))) unsigned int u32x4;
typedef __attribute__((ext_vector_type(4))) float f32x4;

constexpr int NB = 4, D = 512, H = 32, DH = 128, SK = 4096, W = 1024, F = 1376;

__device__ __forceinline__ float bf_lo(u32 v) { return __uint_as_float(v << 16); }
__device__ __forceinline__ float bf_hi(u32 v) { return __uint_as_float(v & 0xffff0000u); }
__device__ __forceinline__ float bf2f(u16 v) { return __uint_as_float(((u32)v) << 16); }
__device__ __forceinline__ u16 f2bf(float f) {
    u32 u = __float_as_uint(f);
    if ((u & 0x7fffffffu) > 0x7f800000u) return (u16)((u >> 16) | 0x40);
    return (u16)((u + 0x7fffu + ((u >> 16) & 1u)) >> 16);
}
__device__ __forceinline__ float wave_sum(float v) {
#pragma unroll
    for (int o = 32; o > 0; o >>= 1) v += __shfl_xor(v, o);
    return v;
}
__device__ __forceinline__ float wave_max(float v) {
#pragma unroll
    for (int o = 32; o > 0; o >>= 1) v = fmaxf(v, __shfl_xor(v, o));
    return v;
}

__device__ __forceinline__ void ln_rows(const float* src  , float* dst, int tid) {
    int w = tid >> 6, l = tid & 63;
    float v[8]; float s = 0.f;
#pragma unroll
    for (int i = 0; i < 8; ++i) { v[i] = src[w * D + l + 64 * i]; s += v[i]; }
    float m = wave_sum(s) * (1.f / D);
    float q = 0.f;
#pragma unroll
    for (int i = 0; i < 8; ++i) { float d = v[i] - m; q += d * d; }
    float var = wave_sum(q) * (1.f / D);
    float r = 1.f / sqrtf(var + 1e-5f);
#pragma unroll
    for (int i = 0; i < 8; ++i) dst[w * D + l + 64 * i] = (v[i] - m) * r;
}

__global__ __launch_bounds__(256) void k_q(const u16* __restrict__ x, const u16* __restrict__ wq, float* __restrict__ q) {
    __shared__ float xs[NB * D];
    __shared__ float red[64 * 4 * 33];
    int tid = threadIdx.x;
    for (int i = tid; i < NB * D; i += 256) xs[i] = bf2f(x[i]);
    __syncthreads();
    ln_rows(xs, xs, tid);
    __syncthreads();
    int n0 = blockIdx.x * 32;
    int c = tid & 3, r = tid >> 2;
    float acc[NB][8];
#pragma unroll
    for (int b = 0; b < NB; ++b)
#pragma unroll
        for (int j = 0; j < 8; ++j) acc[b][j] = 0.f;
    u32x4 wv[8];
#pragma unroll
    for (int i = 0; i < 8; ++i) wv[i] = *(const u32x4*)(wq + (size_t)(r + 64 * i) * (H * DH) + n0 + c * 8);
#pragma unroll
    for (int i = 0; i < 8; ++i) {
#pragma unroll
        for (int b = 0; b < NB; ++b) {
            float xv = xs[b * D + r + 64 * i];
#pragma unroll
            for (int j = 0; j < 4; ++j) {
                acc[b][2 * j] += xv * bf_lo(wv[i][j]);
                acc[b][2 * j + 1] += xv * bf_hi(wv[i][j]);
            }
        }
    }
    for (int b = 0; b < NB; ++b) {
        __syncthreads();
#pragma unroll
        for (int j = 0; j < 8; ++j) red[r * 33 + c * 8 + j] = acc[b][j];
        __syncthreads();
        if (tid < 32) {
            float s = 0.f;
            for (int i = 0; i < 64; ++i) s += red[i * 33 + tid];
            q[b * (H * DH) + n0 + tid] = s;
        }
    }
}

__global__ __launch_bounds__(512) void k_attn(const float* __restrict__ q, const u16* __restrict__ kc, const u16* __restrict__ vc,
                                              float* __restrict__ po, float* __restrict__ pml) {
    __shared__ float sc[512];
    __shared__ float red[8][16 * 8 * 4];
    __shared__ float wm[8], wl[8];
    int tid = threadIdx.x, w = tid >> 6, l = tid & 63;
    int bh = blockIdx.x >> 1, half = blockIdx.x & 1;
    size_t base = ((size_t)bh * SK + (SK - W) + half * 512 + w * 64) * DH;
    const u16* kp = kc + base + l * 8;
    const u16* vp = vc + base + l * 8;
    u32x4 kv[16], vv[16];
#pragma unroll
    for (int i = 0; i < 16; ++i) kv[i] = __builtin_nontemporal_load((const u32x4*)(kp + i * 512));
#pragma unroll
    for (int i = 0; i < 16; ++i) vv[i] = __builtin_nontemporal_load((const u32x4*)(vp + i * 512));
    float qf[8];
    {
        int b = bh >> 5, h = bh & 31;
        const float* qp = q + b * (H * DH) + h * DH + (l & 15) * 8;
#pragma unroll
        for (int j = 0; j < 8; ++j) qf[j] = qp[j] * 0.08838834764831845f;
    }
    float s[16];
#pragma unroll
    for (int i = 0; i < 16; ++i) {
        float a = 0.f;
#pragma unroll
        for (int j = 0; j < 4; ++j) { a += qf[2 * j] * bf_lo(kv[i][j]); a += qf[2 * j + 1] * bf_hi(kv[i][j]); }
#pragma unroll
        for (int o = 8; o > 0; o >>= 1) a += __shfl_xor(a, o);
        s[i] = a;
    }
    float m = s[0];
#pragma unroll
    for (int i = 1; i < 16; ++i) m = fmaxf(m, s[i]);
    m = wave_max(m);
    float lsum = 0.f;
    float acc[8];
#pragma unroll
    for (int j = 0; j < 8; ++j) acc[j] = 0.f;
#pragma unroll
    for (int i = 0; i < 16; ++i) {
        float p = __expf(s[i] - m);
        lsum += p;
#pragma unroll
        for (int j = 0; j < 4; ++j) { acc[2 * j] += p * bf_lo(vv[i][j]); acc[2 * j + 1] += p * bf_hi(vv[i][j]); }
    }
    lsum += __shfl_xor(lsum, 16); lsum += __shfl_xor(lsum, 32);
#pragma unroll
    for (int j = 0; j < 8; ++j) { acc[j] += __shfl_xor(acc[j], 16); acc[j] += __shfl_xor(acc[j], 32); }
    __shared__ float wo_[8][128];
    if (l < 16) {
#pragma unroll
        for (int j = 0; j < 8; ++j) wo_[w][l * 8 + j] = acc[j];
    }
    if (l == 0) { wm[w] = m; wl[w] = lsum; }
    __syncthreads();
    if (tid < 128) {
        float M = wm[0];
#pragma unroll
        for (int i = 1; i < 8; ++i) M = fmaxf(M, wm[i]);
        float L = 0.f, o = 0.f;
#pragma unroll
        for (int i = 0; i < 8; ++i) { float e = __expf(wm[i] - M); L += e * wl[i]; o += e * wo_[i][tid]; }
        po[blockIdx.x * 128 + tid] = o;
        if (tid == 0) { pml[blockIdx.x * 2] = M; pml[blockIdx.x * 2 + 1] = L; }
    }
}

__global__ __launch_bounds__(256) void k_wo(const u16* __restrict__ x, const float* __restrict__ po, const float* __restrict__ pml,
                                            const u16* __restrict__ wo, float* __restrict__ hbuf) {
    __shared__ float os[NB * 4096];
    __shared__ float red[128 * 17];
    int tid = threadIdx.x;
    int n0 = blockIdx.x * 16;
    int c = tid & 1, r = tid >> 1;
    u32x4 wv[32];
#pragma unroll
    for (int i = 0; i < 32; ++i) wv[i] = *(const u32x4*)(wo + (size_t)(r + 128 * i) * D + n0 + c * 8);
    for (int i = tid; i < NB * 4096; i += 256) {
        int b = i >> 12, hh = (i >> 7) & 31, d = i & 127;
        int p = (b * 32 + hh) * 2;
        float m0 = pml[p * 2], l0 = pml[p * 2 + 1], m1 = pml[p * 2 + 2], l1 = pml[p * 2 + 3];
        float M = fmaxf(m0, m1);
        float e0 = __expf(m0 - M), e1 = __expf(m1 - M);
        os[i] = (e0 * po[p * 128 + d] + e1 * po[(p + 1) * 128 + d]) / (e0 * l0 + e1 * l1);
    }
    __syncthreads();
    float acc[NB][8];
#pragma unroll
    for (int b = 0; b < NB; ++b)
#pragma unroll
        for (int j = 0; j < 8; ++j) acc[b][j] = 0.f;
#pragma unroll
    for (int i = 0; i < 32; ++i) {
#pragma unroll
        for (int b = 0; b < NB; ++b) {
            float xv = os[b * 4096 + r + 128 * i];
#pragma unroll
            for (int j = 0; j < 4; ++j) {
                acc[b][2 * j] += xv * bf_lo(wv[i][j]);
                acc[b][2 * j + 1] += xv * bf_hi(wv[i][j]);
            }
        }
    }
    for (int b = 0; b < NB; ++b) {
        __syncthreads();
#pragma unroll
        for (int j = 0; j < 8; ++j) red[r * 17 + c * 8 + j] = acc[b][j];
        __syncthreads();
        if (tid < 16) {
            float s = 0.f;
            for (int i = 0; i < 128; ++i) s += red[i * 17 + tid];
            hbuf[b * D + n0 + tid] = s + bf2f(x[b * D + n0 + tid]);
        }
    }
}

__global__ __launch_bounds__(256) void k_gu(const float* __restrict__ hbuf, const u16* __restrict__ wg, const u16* __restrict__ wu,
                                            float* __restrict__ abuf) {
    __shared__ float xs[NB * D];
    __shared__ float red[64 * 33 * 2];
    int tid = threadIdx.x;
    int n0 = blockIdx.x * 32;
    int c = tid & 3, r = tid >> 2;
    u32x4 gv[8], uv[8];
#pragma unroll
    for (int i = 0; i < 8; ++i) {
        gv[i] = *(const u32x4*)(wg + (size_t)(r + 64 * i) * F + n0 + c * 8);
        uv[i] = *(const u32x4*)(wu + (size_t)(r + 64 * i) * F + n0 + c * 8);
    }
    for (int i = tid; i < NB * D; i += 256) xs[i] = hbuf[i];
    __syncthreads();
    ln_rows(xs, xs, tid);
    __syncthreads();
    float ag[NB][8], au[NB][8];
#pragma unroll
    for (int b = 0; b < NB; ++b)
#pragma unroll
        for (int j = 0; j < 8; ++j) { ag[b][j] = 0.f; au[b][j] = 0.f; }
#pragma unroll
    for (int i = 0; i < 8; ++i) {
#pragma unroll
        for (int b = 0; b < NB; ++b) {
            float xv = xs[b * D + r + 64 * i];
#pragma unroll
            for (int j = 0; j < 4; ++j) {
                ag[b][2 * j] += xv * bf_lo(gv[i][j]);
                ag[b][2 * j + 1] += xv * bf_hi(gv[i][j]);
                au[b][2 * j] += xv * bf_lo(uv[i][j]);
                au[b][2 * j + 1] += xv * bf_hi(uv[i][j]);
            }
        }
    }
    for (int b = 0; b < NB; ++b) {
        __syncthreads();
#pragma unroll
        for (int j = 0; j < 8; ++j) { red[r * 33 + c * 8 + j] = ag[b][j]; red[64 * 33 + r * 33 + c * 8 + j] = au[b][j]; }
        __syncthreads();
        if (tid < 32) {
            float s = 0.f, t = 0.f;
            for (int i = 0; i < 64; ++i) { s += red[i * 33 + tid]; t += red[64 * 33 + i * 33 + tid]; }
            abuf[b * F + n0 + tid] = s * t;
        }
    }
}

__global__ __launch_bounds__(256) void k_down(const float* __restrict__ hbuf, const float* __restrict__ abuf, const u16* __restrict__ wd,
                                              u16* __restrict__ out) {
    __shared__ float as[NB * F];
    __shared__ float red[128 * 17];
    int tid = threadIdx.x;
    int n0 = blockIdx.x * 16;
    int c = tid & 1, r = tid >> 1;
    u32x4 wv[11];
#pragma unroll
    for (int i = 0; i < 11; ++i) {
        int k = r + 128 * i;
        wv[i] = (k < F) ? *(const u32x4*)(wd + (size_t)k * D + n0 + c * 8) : u32x4{0, 0, 0, 0};
    }
    for (int i = tid; i < NB * F; i += 256) as[i] = abuf[i];
    __syncthreads();
    float acc[NB][8];
#pragma unroll
    for (int b = 0; b < NB; ++b)
#pragma unroll
        for (int j = 0; j < 8; ++j) acc[b][j] = 0.f;
#pragma unroll
    for (int i = 0; i < 11; ++i) {
        int k = r + 128 * i;
#pragma unroll
        for (int b = 0; b < NB; ++b) {
            float xv = (k < F) ? as[b * F + k] : 0.f;
#pragma unroll
            for (int j = 0; j < 4; ++j) {
                acc[b][2 * j] += xv * bf_lo(wv[i][j]);
                acc[b][2 * j + 1] += xv * bf_hi(wv[i][j]);
            }
        }
    }
    for (int b = 0; b < NB; ++b) {
        __syncthreads();
#pragma unroll
        for (int j = 0; j < 8; ++j) red[r * 17 + c * 8 + j] = acc[b][j];
        __syncthreads();
        if (tid < 16) {
            float s = 0.f;
            for (int i = 0; i < 128; ++i) s += red[i * 17 + tid];
            out[b * D + n0 + tid] = f2bf(s + hbuf[b * D + n0 + tid]);
        }
    }
}

extern "C" void kernel_launch(void* const* d_in, const int* in_sizes, int n_in,
                              void* d_out, int out_size, void* d_ws, size_t ws_size,
                              hipStream_t stream) {
    const u16* x = (const u16*)d_in[0];
    const u16* kc = (const u16*)d_in[1];
    const u16* vc = (const u16*)d_in[2];
    const u16* wq = (const u16*)d_in[3];
    const u16* wo = (const u16*)d_in[4];
    const u16* wg = (const u16*)d_in[6];
    const u16* wu = (const u16*)d_in[7];
    const u16* wd = (const u16*)d_in[8];
    float* ws = (float*)d_ws;
    float* q = ws;
    float* po = q + NB * 4096;
    float* pml = po + 256 * 128;
    float* hbuf = pml + 512;
    float* abuf = hbuf + NB * D;
    k_q<<<128, 256, 0, stream>>>(x, wq, q);
    k_attn<<<256, 512, 0, stream>>>(q, kc, vc, po, pml);
    k_wo<<<32, 256, 0, stream>>>(x, po, pml, wo, hbuf);
    k_gu<<<43, 256, 0, stream>>>(hbuf, wg, wu, abuf);
    k_down<<<32, 256, 0, stream>>>(hbuf, abuf, wd, (u16*)d_out);
}
```

```cpp
#include <hip/hip_runtime.h>
#include <stdint.h>

typedef unsigned short u16;
typedef unsigned int u32;
typedef unsigned long long u64;
typedef __attribute__((ext_vector_type(2))) u32 u32x2;
typedef __attribute__((ext_vector_type(4))) u32 u32x4;
typedef __attribute__((ext_vector_type(2))) float f32x2;
typedef __attribute__((ext_vector_type(4))) float f32x4;
typedef __attribute__((ext_vector_type(2))) __bf16 bf16x2;
typedef __attribute__((ext_vector_type(8))) __bf16 bf16x8;
typedef __amdgpu_buffer_rsrc_t rsrc_t;

constexpr int NB = 4, D = 512, H = 32, DH = 128, SK = 4096, W = 1024, F = 1376;
constexpr int NWG = 256, NRED = 32, NMLP = 43;

constexpr u32 OFF_E = 0;
constexpr u32 OFF_FLAG = 1024;
constexpr u32 OFF_ML = 4096;
constexpr u32 OFF_H = 8192;
constexpr u32 OFF_Y = 16384;
constexpr u32 OFF_D = OFF_Y + NWG * 2048;
constexpr u32 WS_USED = OFF_D + NMLP * 8192;
constexpr u32 OFF_TS = 1u << 20;

constexpr u32 SENT = 0xFFFFFFFFu;
constexpr int AUX_NT = 2, AUX_SC1 = 16;
constexpr u32 SPIN_CAP = 1u << 18;

__device__ __forceinline__ rsrc_t mk_rsrc(const void* p, u32 bytes) {
    return __builtin_amdgcn_make_buffer_rsrc((void*)p, 0, (int)bytes, 0x00020000);
}
__device__ __forceinline__ u32x4 ld16(rsrc_t r, u32 voff, u32 soff, const int aux_nt) {
    return aux_nt ? __builtin_amdgcn_raw_buffer_load_b128(r, voff, soff, AUX_NT)
                  : __builtin_amdgcn_raw_buffer_load_b128(r, voff, soff, 0);
}
__device__ __forceinline__ u32x4 ld16_sc1(rsrc_t r, u32 voff) { return __builtin_amdgcn_raw_buffer_load_b128(r, voff, 0, AUX_SC1); }
__device__ __forceinline__ void st16_sc1(u32x4 v, rsrc_t r, u32 voff) { __builtin_amdgcn_raw_buffer_store_b128(v, r, voff, 0, AUX_SC1); }
__device__ __forceinline__ void st8_sc1(u32x2 v, rsrc_t r, u32 voff) { __builtin_amdgcn_raw_buffer_store_b64(v, r, voff, 0, AUX_SC1); }

__device__ __forceinline__ float bf_lo(u32 v) { return __uint_as_float(v << 16); }
__device__ __forceinline__ float bf_hi(u32 v) { return __uint_as_float(v & 0xffff0000u); }
__device__ __forceinline__ u32 pk_bf16(float a, float b) {
    f32x2 v = {a, b};
    return __builtin_bit_cast(u32, __builtin_convertvector(v, bf16x2));
}
__device__ __forceinline__ float bfr(float a) { return bf_lo(pk_bf16(a, a)); }
__device__ __forceinline__ u32 canon(u32 v) { return v < SENT ? v : SENT - 1; }
__device__ __forceinline__ u32x4 canon4(f32x4 v) {
    u32x4 r;
#pragma unroll
    for (int i = 0; i < 4; ++i) r[i] = canon(__float_as_uint(v[i]));
    return r;
}
__device__ __forceinline__ bool full4(u32x4 v) { return v[0] != SENT && v[1] != SENT && v[2] != SENT && v[3] != SENT; }

template <int CTRL> __device__ __forceinline__ float dpp(float v) {
    return __uint_as_float(__builtin_amdgcn_update_dpp(0u, __float_as_uint(v), CTRL, 0xf, 0xf, true));
}
template <int CTRL> __device__ __forceinline__ u32 dppu(u32 v) { return __builtin_amdgcn_update_dpp(0u, v, CTRL, 0xf, 0xf, true); }
constexpr int DPP_X1 = 0xB1, DPP_X2 = 0x4E, DPP_HMIR = 0x141, DPP_MIR = 0x140;
constexpr int DPP_SHL1 = 0x101, DPP_SHL2 = 0x102, DPP_SHL4 = 0x104, DPP_SHL6 = 0x106;

__device__ __forceinline__ float row8_sum(float v) { v += dpp<DPP_X1>(v); v += dpp<DPP_X2>(v); v += dpp<DPP_HMIR>(v); return v; }
__device__ __forceinline__ float row16_sum(float v) { v = row8_sum(v); v += dpp<DPP_MIR>(v); return v; }
__device__ __forceinline__ float xrow_sum(float x) {
    auto s = __builtin_amdgcn_permlane16_swap(__float_as_uint(x), __float_as_uint(x), false, false);
    x = __uint_as_float(s[0]) + __uint_as_float(s[1]);
    auto t = __builtin_amdgcn_permlane32_swap(__float_as_uint(x), __float_as_uint(x), false, false);
    return __uint_as_float(t[0]) + __uint_as_float(t[1]);
}
__device__ __forceinline__ float xrow_max(float x) {
    auto s = __builtin_amdgcn_permlane16_swap(__float_as_uint(x), __float_as_uint(x), false, false);
    x = fmaxf(__uint_as_float(s[0]), __uint_as_float(s[1]));
    auto t = __builtin_amdgcn_permlane32_swap(__float_as_uint(x), __float_as_uint(x), false, false);
    return fmaxf(__uint_as_float(t[0]), __uint_as_float(t[1]));
}
__device__ __forceinline__ float wave_sum(float v) { return xrow_sum(row16_sum(v)); }

__device__ __forceinline__ void lds_barrier() { asm volatile("s_waitcnt lgkmcnt(0)\n\ts_barrier" ::: "memory"); }
__device__ __forceinline__ void pin() { __builtin_amdgcn_sched_barrier(0); }

__device__ __forceinline__ void unpack8(u32x4 v, float* f) {
#pragma unroll
    for (int j = 0; j < 4; ++j) { f[2 * j] = bf_lo(v[j]); f[2 * j + 1] = bf_hi(v[j]); }
}

__device__ __forceinline__ void ln_stats(u32x4 row, float& mb, float& rs) {
    float f[8];
    unpack8(row, f);
    float s = ((f[0] + f[1]) + (f[2] + f[3])) + ((f[4] + f[5]) + (f[6] + f[7]));
    float mean = wave_sum(s) * (1.f / D);
    float q = 0.f;
#pragma unroll
    for (int i = 0; i < 8; ++i) { float d = f[i] - mean; q += d * d; }
    float var = wave_sum(q) * (1.f / D);
    mb = bfr(mean);
    float sq = bfr(__builtin_amdgcn_sqrtf(bfr(bfr(var) + 1e-5f)));
    rs = __builtin_amdgcn_rcpf(sq);
}
__device__ __forceinline__ u32x4 ln_apply(u32x4 v, float mb, float rs) {
    u32x4 r;
#pragma unroll
    for (int j = 0; j < 4; ++j) {
        u32 d = pk_bf16(bf_lo(v[j]) - mb, bf_hi(v[j]) - mb);
        r[j] = pk_bf16(bf_lo(d) * rs, bf_hi(d) * rs);
    }
    return r;
}

__device__ __forceinline__ bf16x8 colfrag(const u32x4* in, int c) {
    const u32 sel = (c & 1) ? 0x07060302u : 0x05040100u;
    u32x4 r;
#pragma unroll
    for (int q = 0; q < 4; ++q) r[q] = __builtin_amdgcn_perm(in[2 * q + 1][c >> 1], in[2 * q][c >> 1], sel);
    return __builtin_bit_cast(bf16x8, r);
}
__device__ __forceinline__ float dot2(u32 a, u32 b, float c) {
    return __builtin_amdgcn_fdot2_f32_bf16(__builtin_bit_cast(bf16x2, a), __builtin_bit_cast(bf16x2, b), c, false);
}
__device__ __forceinline__ f32x4 mfma(bf16x8 a, bf16x8 b, f32x4 c) { return __builtin_amdgcn_mfma_f32_16x16x32_bf16(a, b, c, 0, 0, 0); }
__device__ __forceinline__ bf16x8 asfrag(u32x4 v) { return __builtin_bit_cast(bf16x8, v); }

#define STAMP(k) do { } while (0)

__global__ __launch_bounds__(1024) void fused_block(const u16* __restrict__ x, const u16* __restrict__ kc, const u16* __restrict__ vc,
                                                    const u16* __restrict__ wq, const u16* __restrict__ wo, const u16* __restrict__ wg_,
                                                    const u16* __restrict__ wu, const u16* __restrict__ wd, u16* __restrict__ out,
                                                    char* __restrict__ ws) {
    __shared__ __attribute__((aligned(16))) float s_q[16 * 132];
    __shared__ __attribute__((aligned(16))) float s_o[16 * 132];
    __shared__ __attribute__((aligned(16))) float s_y[4 * 512];
    __shared__ __attribute__((aligned(16))) u16 s_qb[128];
    __shared__ __attribute__((aligned(16))) u16 s_ob[128];
    __shared__ __attribute__((aligned(16))) float s_m[16];
    __shared__ __attribute__((aligned(16))) float s_l[16];
    __shared__ __attribute__((aligned(16))) float s_r[64 * 20];
    __shared__ __attribute__((aligned(16))) float s_r2[64 * 20];
    __shared__ __attribute__((aligned(16))) u16 s_hn[4 * 520];
    __shared__ __attribute__((aligned(16))) float s_gu[2 * 8 * 32 * 4];
    __shared__ __attribute__((aligned(16))) u16 s_a[4 * 32];

    const int t = threadIdx.x, l = t & 63, g = l >> 4, n = l & 15;
    const int w = __builtin_amdgcn_readfirstlane(t >> 6);
    const int wg = blockIdx.x;
    const int h = (wg & 7) * 4 + (wg >> 6), jj = (wg >> 3) & 7, b = jj >> 1, half = jj & 1;
    const int bh = b * H + h, ys = bh * 2 + half;
    const bool is_red = wg < NRED, is_mlp = wg >= NRED && wg < NRED + NMLP;
    const int mj = wg - NRED;

    const rsrc_t r_ws = mk_rsrc(ws, WS_USED);
    const rsrc_t r_x = mk_rsrc(x, NB * D * 2);
    STAMP(0);

    u32x2 ev = {0u, 0u};
    if (w == 15) {
        ev = __builtin_amdgcn_raw_buffer_load_b64(r_ws, OFF_E, 0, AUX_SC1);
        const u32x4 s4 = {SENT, SENT, SENT, SENT};
        st16_sc1(s4, r_ws, OFF_Y + ys * 2048 + l * 16);
        st16_sc1(s4, r_ws, OFF_Y + ys * 2048 + 1024 + l * 16);
        if (l == 0) st8_sc1(u32x2{SENT, SENT}, r_ws, OFF_ML + ys * 8);
        if (is_red && l < 8) st16_sc1(s4, r_ws, OFF_H + wg * 128 + l * 16);
        if (is_mlp) {
#pragma unroll
            for (int i = 0; i < 8; ++i) st16_sc1(s4, r_ws, OFF_D + mj * 8192 + i * 1024 + l * 16);
        }
    }
    pin();

    const u32x4 xrow = ld16(r_x, b * 1024 + l * 16, 0, 0);
    const u32x4 xfr = ld16(r_x, b * 1024 + (4 * w + g) * 16, 0, 0);
    u32x4 wv[8], kv[8], vv[8];
    {
        const rsrc_t r = mk_rsrc(wq + h * DH, (D * H * DH - h * DH) * 2);
        const u32 o = (u32)(32 * w + g * 8) * 8192u + n * 16;
#pragma unroll
        for (int i = 0; i < 8; ++i) wv[i] = ld16(r, o, i * 8192, 1);
    }
    pin();
    asm volatile("s_barrier" ::: "memory");
    pin();
    const size_t kvbase = ((size_t)bh * SK + (SK - W) + half * 512) * DH;
    const u32 kvo = w * 8192 + l * 16;
    {
        const rsrc_t r = mk_rsrc(kc + kvbase, 512 * DH * 2);
#pragma unroll
        for (int i = 0; i < 8; ++i) kv[i] = ld16(r, kvo + (i & 3) * 1024, (i >> 2) * 4096, 1);
    }
    pin();

    {
        float mb, rs;
        ln_stats(xrow, mb, rs);
        const bf16x8 xa = asfrag(ln_apply(xfr, mb, rs));
        STAMP(1);
        float qp[8];
#pragma unroll
        for (int c = 0; c < 8; ++c) qp[c] = mfma(xa, colfrag(wv, c), f32x4{0.f, 0.f, 0.f, 0.f})[0];
        if (l < 16) {
            *(f32x4*)&s_q[w * 132 + n * 8] = f32x4{qp[0], qp[1], qp[2], qp[3]};
            *(f32x4*)&s_q[w * 132 + n * 8 + 4] = f32x4{qp[4], qp[5], qp[6], qp[7]};
        }
    }
    pin();
    STAMP(2);

    u32 tg0 = 0, tg1 = 0;
    if (w == 15) {
        asm volatile("s_waitcnt vmcnt(8)" ::: "memory");
        const u64 e = ((u64)(u32)__builtin_amdgcn_readfirstlane(ev[1]) << 32) | (u32)__builtin_amdgcn_readfirstlane(ev[0]);
        const u64 tag = e * 6364136223846793005ull + 1442695040888963407ull;
        tg0 = (u32)tag; tg1 = (u32)(tag >> 32);
        if (l == 0) st8_sc1(u32x2{tg0, tg1}, r_ws, OFF_FLAG + wg * 8);
    }
    pin();

    const int cg = w & 3, kq = w >> 2;
    {
        const rsrc_t r = mk_rsrc(wo + (size_t)h * DH * D, DH * D * 2);
        const u32 o = (u32)(kq * 32 + g * 8) * 1024u + cg * 256 + n * 16;
#pragma unroll
        for (int i = 0; i < 8; ++i) wv[i] = ld16(r, o + (i & 3) * 1024, (i >> 2) * 4096, 1);
    }
    {
        const rsrc_t r = mk_rsrc(vc + kvbase, 512 * DH * 2);
#pragma unroll
        for (int i = 0; i < 8; ++i) vv[i] = ld16(r, kvo + (i & 3) * 1024, (i >> 2) * 4096, 1);
    }
    u32x4 fl0 = {0, 0, 0, 0}, fl1 = {0, 0, 0, 0};
    const bool chk = (w == 15) && (is_red || is_mlp);
    if (chk) { fl0 = ld16_sc1(r_ws, OFF_FLAG + l * 16); fl1 = ld16_sc1(r_ws, OFF_FLAG + 1024 + l * 16); }
    pin();

    lds_barrier();
    {
        const int d = t >> 3, s = t & 7;
        float v = s_q[(2 * s) * 132 + d] + s_q[(2 * s + 1) * 132 + d];
        v = row8_sum(v);
        if (s == 0) s_qb[d] = (u16)pk_bf16(v, v);
    }
    lds_barrier();
    const u32x4 qf = *(const u32x4*)&s_qb[n * 8];
    STAMP(3);

    float sc[8];
#pragma unroll
    for (int i = 0; i < 8; ++i) {
        float a = 0.f;
#pragma unroll
        for (int j = 0; j < 4; ++j)
            a = dot2(kv[i][j], qf[j], a);
        sc[i] = row16_sum(a);
    }
#pragma unroll
    for (int i = 0; i < 8; i += 2) {
        u32 p = pk_bf16(sc[i], sc[i + 1]);
        p = pk_bf16(bf_lo(p) * 0.08838834764831845f, bf_hi(p) * 0.08838834764831845f);
        sc[i] = bf_lo(p); sc[i + 1] = bf_hi(p);
    }
    float mx = fmaxf(fmaxf(fmaxf(sc[0], sc[1]), fmaxf(sc[2], sc[3])), fmaxf(fmaxf(sc[4], sc[5]), fmaxf(sc[6], sc[7])));
    mx = xrow_max(mx);
    if (l == 0) s_m[w] = mx;
    STAMP(4);
    lds_barrier();
    float M;
    {
        const f32x4 a = *(const f32x4*)&s_m[0], bb = *(const f32x4*)&s_m[4], c = *(const f32x4*)&s_m[8], d = *(const f32x4*)&s_m[12];
        M = fmaxf(fmaxf(fmaxf(fmaxf(a[0], a[1]), fmaxf(a[2], a[3])), fmaxf(fmaxf(bb[0], bb[1]), fmaxf(bb[2], bb[3]))),
                  fmaxf(fmaxf(fmaxf(c[0], c[1]), fmaxf(c[2], c[3])), fmaxf(fmaxf(d[0], d[1]), fmaxf(d[2], d[3]))));
    }
    u32x4 pa;
    {
        float ls = 0.f;
#pragma unroll
        for (int i = 0; i < 8; i += 2) {
            const float p0 = __builtin_amdgcn_exp2f((sc[i] - M) * 1.4426950408889634f);
            const float p1 = __builtin_amdgcn_exp2f((sc[i + 1] - M) * 1.4426950408889634f);
            ls += p0 + p1;
            pa[i >> 1] = pk_bf16(p0, p1);
        }
        ls = xrow_sum(ls);
        if (l == 0) s_l[w] = ls;
    }
    bf16x8 wf[8];
#pragma unroll
    for (int c = 0; c < 8; ++c) wf[c] = colfrag(wv, c);
    pin();
    STAMP(5);

    {
        float op[8];
#pragma unroll
        for (int c = 0; c < 8; ++c) op[c] = mfma(asfrag(pa), colfrag(vv, c), f32x4{0.f, 0.f, 0.f, 0.f})[0];
        if (l < 16) {
            *(f32x4*)&s_o[w * 132 + n * 8] = f32x4{op[0], op[1], op[2], op[3]};
            *(f32x4*)&s_o[w * 132 + n * 8 + 4] = f32x4{op[4], op[5], op[6], op[7]};
        }
    }
    STAMP(6);
    if (chk) {
        for (u32 spin = 0; spin < SPIN_CAP; ++spin) {
            const bool ok = fl0[0] == tg0 && fl0[1] == tg1 && fl0[2] == tg0 && fl0[3] == tg1 &&
                            fl1[0] == tg0 && fl1[1] == tg1 && fl1[2] == tg0 && fl1[3] == tg1;
            if (__all(ok)) break;
            asm volatile("" ::: "memory");
            fl0 = ld16_sc1(r_ws, OFF_FLAG + l * 16); fl1 = ld16_sc1(r_ws, OFF_FLAG + 1024 + l * 16);
        }
        if (wg == 0 && l == 0) st8_sc1(u32x2{tg0, tg1}, r_ws, OFF_E);
    }
    lds_barrier();
    float L;
    {
        const f32x4 a = *(const f32x4*)&s_l[0], bb = *(const f32x4*)&s_l[4], c = *(const f32x4*)&s_l[8], d = *(const f32x4*)&s_l[12];
        const f32x4 e = (a + bb) + (c + d);
        L = (e[0] + e[1]) + (e[2] + e[3]);
        const int d0 = t >> 3, s = t & 7;
        float v = s_o[(2 * s) * 132 + d0] + s_o[(2 * s + 1) * 132 + d0];
        v = row8_sum(v) * __builtin_amdgcn_rcpf(L);
        if (s == 0) s_ob[d0] = (u16)pk_bf16(v, v);
    }
    lds_barrier();
    {
        const bf16x8 oa = asfrag(*(const u32x4*)&s_ob[kq * 32 + g * 8]);
        float yp[8];
#pragma unroll
        for (int c = 0; c < 8; ++c) yp[c] = mfma(oa, wf[c], f32x4{0.f, 0.f, 0.f, 0.f})[0];
        if (l < 16) {
            *(f32x4*)&s_y[kq * 512 + cg * 128 + n * 8] = f32x4{yp[0], yp[1], yp[2], yp[3]};
            *(f32x4*)&s_y[kq * 512 + cg * 128 + n * 8 + 4] = f32x4{yp[4], yp[5], yp[6], yp[7]};
        }
    }
    lds_barrier();
    if (w < 2) {
        const f32x4 a = *(const f32x4*)&s_y[t * 4], bb = *(const f32x4*)&s_y[512 + t * 4];
        const f32x4 c = *(const f32x4*)&s_y[1024 + t * 4], d = *(const f32x4*)&s_y[1536 + t * 4];
        st16_sc1(canon4((a + bb) + (c + d)), r_ws, OFF_Y + ys * 2048 + t * 16);
        if (t == 0) st8_sc1(u32x2{canon(__float_as_uint(M)), canon(__float_as_uint(L))}, r_ws, OFF_ML + ys * 8);
    }
    pin();
    STAMP(7);

    if (is_red) {
        const int rb = wg >> 3, ch = wg & 7;
        float xres = 0.f;
        if (w == 0) xres = __uint_as_float((u32)x[rb * D + ch * 64 + l] << 16);
        const int p = w * 4 + g;
        const u32 yo = OFF_Y + (u32)(rb * 64 + p) * 2048u + ch * 256 + n * 16;
        const u32 mo = OFF_ML + (u32)(rb * 32 + (p >> 1)) * 16u;
        u32x4 yv, ml;
        for (u32 spin = 0; spin < SPIN_CAP; ++spin) {
            asm volatile("" ::: "memory");
            yv = ld16_sc1(r_ws, yo);
            ml = ld16_sc1(r_ws, mo);
            if (__all(full4(yv) && full4(ml))) break;
        }
        STAMP(8);
        {
            const float m0 = __uint_as_float(ml[0]), l0 = __uint_as_float(ml[1]), m1 = __uint_as_float(ml[2]), l1 = __uint_as_float(ml[3]);
            const float mm = fmaxf(m0, m1);
            const float e0 = l0 * __builtin_amdgcn_exp2f((m0 - mm) * 1.4426950408889634f);
            const float e1 = l1 * __builtin_amdgcn_exp2f((m1 - mm) * 1.4426950408889634f);
            const float wt = ((p & 1) ? e1 : e0) * __builtin_amdgcn_rcpf(e0 + e1);
            float v[4];
#pragma unroll
            for (int i = 0; i < 4; ++i) v[i] = xrow_sum(__uint_as_float(yv[i]) * wt);
            if (l < 16) {
#pragma unroll
                for (int i = 0; i < 4; ++i) s_r[(n * 4 + i) * 20 + w] = v[i];
            }
        }
        lds_barrier();
        float hval = 0.f;
        if (w == 0) {
            const f32x4 a = *(const f32x4*)&s_r[l * 20], bb = *(const f32x4*)&s_r[l * 20 + 4];
            const f32x4 c = *(const f32x4*)&s_r[l * 20 + 8], d = *(const f32x4*)&s_r[l * 20 + 12];
            const f32x4 e = (a + bb) + (c + d);
            hval = bfr(xres + bfr((e[0] + e[1]) + (e[2] + e[3])));
            const u32 pr = __builtin_amdgcn_perm(dppu<DPP_SHL1>(__float_as_uint(hval)), __float_as_uint(hval), 0x07060302u);
            const u32x4 hv = {canon(pr), canon(dppu<DPP_SHL2>(pr)), canon(dppu<DPP_SHL4>(pr)), canon(dppu<DPP_SHL6>(pr))};
            if ((l & 7) == 0) st16_sc1(hv, r_ws, OFF_H + wg * 128 + l * 2);
        }
        STAMP(9);
        if (w < 11) {
            const int pj = w * 4 + g;
            const int pjc = pj < NMLP ? pj : NMLP - 1;
            const u32 o = OFF_D + (u32)pjc * 8192u + (ch >> 1) * 2048 + (n >> 3) * 1024 + rb * 256 + (ch & 1) * 128 + (n & 7) * 16;
            u32x4 dv;
            for (u32 spin = 0; spin < SPIN_CAP; ++spin) {
                asm volatile("" ::: "memory");
                dv = ld16_sc1(r_ws, o);
                if (__all(full4(dv))) break;
            }
            STAMP(10);
            float v[4];
#pragma unroll
            for (int i = 0; i < 4; ++i) v[i] = xrow_sum(pj < NMLP ? __uint_as_float(dv[i]) : 0.f);
            if (l < 16) {
#pragma unroll
                for (int i = 0; i < 4; ++i) s_r2[((n & 7) * 8 + (n >> 3) * 4 + i) * 20 + w] = v[i];
            }
        }
        lds_barrier();
        if (w == 0) {
            const f32x4 a = *(const f32x4*)&s_r2[l * 20], bb = *(const f32x4*)&s_r2[l * 20 + 4], c = *(const f32x4*)&s_r2[l * 20 + 8];
            const f32x4 e = a + bb;
            const float sum = ((e[0] + e[1]) + (e[2] + e[3])) + ((c[0] + c[1]) + c[2]);
            const float o = hval + bfr(sum);
            out[rb * D + ch * 64 + l] = (u16)pk_bf16(o, o);
        }
        STAMP(11);
    } else if (is_mlp) {
        if (w < 4) {
            const int mat = w & 1, kh = w >> 1, kb = n >> 2, chunk = n & 3;
            const rsrc_t r = mk_rsrc(mat ? wu : wg_, D * F * 2);
            const u32 o = (u32)(kh * 256 + kb * 32 + g * 8) * (F * 2) + mj * 64 + chunk * 16;
            u32x4 w0[8], w1[8];
#pragma unroll
            for (int i = 0; i < 8; ++i) w0[i] = ld16(r, o, i * (F * 2), 1);
#pragma unroll
            for (int i = 0; i < 8; ++i) w1[i] = ld16(r, o, (128 + i) * (F * 2), 1);
            bf16x8 f0[8], f1[8];
#pragma unroll
            for (int c = 0; c < 8; ++c) { f0[c] = colfrag(w0, c); }
#pragma unroll
            for (int c = 0; c < 8; ++c) { f1[c] = colfrag(w1, c); }
            pin();
            lds_barrier();
            const int ab = n & 3, akb = n >> 2;
            const bf16x8 a0 = asfrag(*(const u32x4*)&s_hn[ab * 520 + kh * 256 + akb * 32 + g * 8]);
            const bf16x8 a1 = asfrag(*(const u32x4*)&s_hn[ab * 520 + kh * 256 + 128 + akb * 32 + g * 8]);
            f32x4 acc[8];
#pragma unroll
            for (int c = 0; c < 8; ++c) acc[c] = mfma(a0, f0[c], f32x4{0.f, 0.f, 0.f, 0.f});
#pragma unroll
            for (int c = 0; c < 8; ++c) acc[c] = mfma(a1, f1[c], acc[c]);
            if (kb == g) {
#pragma unroll
                for (int c = 0; c < 8; ++c) *(f32x4*)&s_gu[((mat * 8 + kh * 4 + kb) * 32 + chunk * 8 + c) * 4] = acc[c];
            }
            lds_barrier();
            if (w == 0) {
                f32x4 s = {0.f, 0.f, 0.f, 0.f};
                const int m2 = l >> 5, col = l & 31;
#pragma unroll
                for (int i = 0; i < 8; ++i) s += *(const f32x4*)&s_gu[((m2 * 8 + i) * 32 + col) * 4];
#pragma unroll
                for (int i = 0; i < 4; ++i) {
                    const float v = bfr(s[i]);
                    auto sw = __builtin_amdgcn_permlane32_swap(__float_as_uint(v), __float_as_uint(v), false, false);
                    const float pr = __uint_as_float(sw[0]) * __uint_as_float(sw[1]);
                    if (l < 32) s_a[i * 32 + col] = (u16)pk_bf16(pr, pr);
                }
            }
            lds_barrier();
        } else if (w < 8) {
            const int c4 = w - 4;
            const rsrc_t r = mk_rsrc(wd + (size_t)mj * 32 * D, 32 * D * 2);
            const u32 o = (u32)(g * 8) * 1024u + c4 * 256 + n * 16;
            u32x4 w0[8];
#pragma unroll
            for (int i = 0; i < 8; ++i) w0[i] = ld16(r, o + (i & 3) * 1024, (i >> 2) * 4096, 1);
            bf16x8 f0[8];
#pragma unroll
            for (int c = 0; c < 8; ++c) f0[c] = colfrag(w0, c);
            pin();
            lds_barrier();
            lds_barrier();
            lds_barrier();
            const bf16x8 aa = asfrag(*(const u32x4*)&s_a[(n >> 2) * 32 + g * 8]);
            float dp[8];
#pragma unroll
            for (int c = 0; c < 8; ++c) dp[c] = mfma(aa, f0[c], f32x4{0.f, 0.f, 0.f, 0.f})[0];
            const u32 so = OFF_D + (u32)mj * 8192u + c4 * 2048 + l * 16;
            st16_sc1(canon4(f32x4{dp[0], dp[1], dp[2], dp[3]}), r_ws, so);
            st16_sc1(canon4(f32x4{dp[4], dp[5], dp[6], dp[7]}), r_ws, so + 1024);
            STAMP(11);
        } else if (w >= 12) {
            const int hb = w - 12;
            u32x4 hv;
            for (u32 spin = 0; spin < SPIN_CAP; ++spin) {
                asm volatile("" ::: "memory");
                hv = ld16_sc1(r_ws, OFF_H + hb * 1024 + l * 16);
                if (__all(full4(hv))) break;
            }
            STAMP(8);
            float mb, rs;
            ln_stats(hv, mb, rs);
            *(u32x4*)&s_hn[hb * 520 + l * 8] = ln_apply(hv, mb, rs);
            STAMP(9);
            lds_barrier();
            lds_barrier();
            lds_barrier();
        } else {
            lds_barrier();
            lds_barrier();
            lds_barrier();
        }
    }
    STAMP(12);
}

extern "C" void kernel_launch(void* const* d_in, const int* in_sizes, int n_in, void* d_out, int out_size, void* d_ws,
                              size_t ws_size, hipStream_t stream) {
    fused_block<<<NWG, 1024, 0, stream>>>((const u16*)d_in[0], (const u16*)d_in[1], (const u16*)d_in[2], (const u16*)d_in[3],
                                          (const u16*)d_in[4], (const u16*)d_in[6], (const u16*)d_in[7], (const u16*)d_in[8],
                                          (u16*)d_out, (char*)d_ws);
}
```

```cpp
#include <hip/hip_runtime.h>
#include <stdint.h>
#include <stdio.h>

typedef unsigned short u16;
typedef unsigned int u32;
typedef unsigned long long u64;
typedef __attribute__((ext_vector_type(2))) u32 u32x2;
typedef __attribute__((ext_vector_type(4))) u32 u32x4;
typedef __attribute__((ext_vector_type(2))) float f32x2;
typedef __attribute__((ext_vector_type(4))) float f32x4;
typedef __attribute__((ext_vector_type(2))) __bf16 bf16x2;
typedef __attribute__((ext_vector_type(8))) __bf16 bf16x8;
typedef __amdgpu_buffer_rsrc_t rsrc_t;

constexpr int NB = 4, D = 512, H = 32, DH = 128, SK = 4096, W = 1024, F = 1376;
constexpr int NWG = 256, NRED = 32, NMLP = 43;

constexpr u32 OFF_E = 0;
constexpr u32 OFF_FLAG = 1024;
constexpr u32 OFF_ML = 4096;
constexpr u32 OFF_H = 8192;
constexpr u32 OFF_Y = 16384;
constexpr u32 OFF_D = OFF_Y + NWG * 2048;
constexpr u32 WS_USED = OFF_D + NMLP * 8192;
constexpr u32 OFF_TS = 1u << 20;

constexpr u32 SENT = 0xFFFFFFFFu;
constexpr int AUX_NT = 2, AUX_SC1 = 16;
constexpr u32 SPIN_CAP = 1u << 18;

__device__ __forceinline__ rsrc_t mk_rsrc(const void* p, u32 bytes) {
    return __builtin_amdgcn_make_buffer_rsrc((void*)p, 0, (int)bytes, 0x00020000);
}
#define WQ_AUX 2
#define WO_AUX 2
template <int AUX> __device__ __forceinline__ u32x4 ldx(rsrc_t r, u32 voff, u32 soff) { return __builtin_amdgcn_raw_buffer_load_b128(r, voff, soff, AUX); }
__device__ __forceinline__ u32x4 ld16(rsrc_t r, u32 voff, u32 soff, const int aux_nt) {
    return aux_nt ? __builtin_amdgcn_raw_buffer_load_b128(r, voff, soff, AUX_NT)
                  : __builtin_amdgcn_raw_buffer_load_b128(r, voff, soff, 0);
}
__device__ __forceinline__ u32x4 ld16_sc1(rsrc_t r, u32 voff) { return __builtin_amdgcn_raw_buffer_load_b128(r, voff, 0, AUX_SC1); }
__device__ __forceinline__ void st16_sc1(u32x4 v, rsrc_t r, u32 voff) { __builtin_amdgcn_raw_buffer_store_b128(v, r, voff, 0, AUX_SC1); }
__device__ __forceinline__ void st8_sc1(u32x2 v, rsrc_t r, u32 voff) { __builtin_amdgcn_raw_buffer_store_b64(v, r, voff, 0, AUX_SC1); }

__device__ __forceinline__ float bf_lo(u32 v) { return __uint_as_float(v << 16); }
__device__ __forceinline__ float bf_hi(u32 v) { return __uint_as_float(v & 0xffff0000u); }
__device__ __forceinline__ u32 pk_bf16(float a, float b) {
    f32x2 v = {a, b};
    return __builtin_bit_cast(u32, __builtin_convertvector(v, bf16x2));
}
__device__ __forceinline__ float bfr(float a) { return bf_lo(pk_bf16(a, a)); }
__device__ __forceinline__ u32 canon(u32 v) { return v < SENT ? v : SENT - 1; }
__device__ __forceinline__ u32x4 canon4(f32x4 v) {
    u32x4 r;
#pragma unroll
    for (int i = 0; i < 4; ++i) r[i] = canon(__float_as_uint(v[i]));
    return r;
}
__device__ __forceinline__ bool full4(u32x4 v) { return v[0] != SENT && v[1] != SENT && v[2] != SENT && v[3] != SENT; }

template <int CTRL> __device__ __forceinline__ float dpp(float v) {
    return __uint_as_float(__builtin_amdgcn_update_dpp(0u, __float_as_uint(v), CTRL, 0xf, 0xf, true));
}
template <int CTRL> __device__ __forceinline__ u32 dppu(u32 v) { return __builtin_amdgcn_update_dpp(0u, v, CTRL, 0xf, 0xf, true); }
constexpr int DPP_X1 = 0xB1, DPP_X2 = 0x4E, DPP_HMIR = 0x141, DPP_MIR = 0x140;
constexpr int DPP_SHL1 = 0x101, DPP_SHL2 = 0x102, DPP_SHL4 = 0x104, DPP_SHL6 = 0x106;

__device__ __forceinline__ float row8_sum(float v) { v += dpp<DPP_X1>(v); v += dpp<DPP_X2>(v); v += dpp<DPP_HMIR>(v); return v; }
__device__ __forceinline__ float row16_sum(float v) { v = row8_sum(v); v += dpp<DPP_MIR>(v); return v; }
__device__ __forceinline__ float xrow_sum(float x) {
    auto s = __builtin_amdgcn_permlane16_swap(__float_as_uint(x), __float_as_uint(x), false, false);
    x = __uint_as_float(s[0]) + __uint_as_float(s[1]);
    auto t = __builtin_amdgcn_permlane32_swap(__float_as_uint(x), __float_as_uint(x), false, false);
    return __uint_as_float(t[0]) + __uint_as_float(t[1]);
}
__device__ __forceinline__ float xrow_max(float x) {
    auto s = __builtin_amdgcn_permlane16_swap(__float_as_uint(x), __float_as_uint(x), false, false);
    x = fmaxf(__uint_as_float(s[0]), __uint_as_float(s[1]));
    auto t = __builtin_amdgcn_permlane32_swap(__float_as_uint(x), __float_as_uint(x), false, false);
    return fmaxf(__uint_as_float(t[0]), __uint_as_float(t[1]));
}
__device__ __forceinline__ float wave_sum(float v) { return xrow_sum(row16_sum(v)); }

__device__ __forceinline__ void lds_barrier() { asm volatile("s_waitcnt lgkmcnt(0)\n\ts_barrier" ::: "memory"); }
__device__ __forceinline__ void pin() { __builtin_amdgcn_sched_barrier(0); }

__device__ __forceinline__ void unpack8(u32x4 v, float* f) {
#pragma unroll
    for (int j = 0; j < 4; ++j) { f[2 * j] = bf_lo(v[j]); f[2 * j + 1] = bf_hi(v[j]); }
}

__device__ __forceinline__ void ln_stats(u32x4 row, float& mb, float& rs) {
    float f[8];
    unpack8(row, f);
    float s = ((f[0] + f[1]) + (f[2] + f[3])) + ((f[4] + f[5]) + (f[6] + f[7]));
    float mean = wave_sum(s) * (1.f / D);
    float q = 0.f;
#pragma unroll
    for (int i = 0; i < 8; ++i) { float d = f[i] - mean; q += d * d; }
    float var = wave_sum(q) * (1.f / D);
    mb = bfr(mean);
    float sq = bfr(__builtin_amdgcn_sqrtf(bfr(bfr(var) + 1e-5f)));
    rs = __builtin_amdgcn_rcpf(sq);
}
__device__ __forceinline__ u32x4 ln_apply(u32x4 v, float mb, float rs) {
    u32x4 r;
#pragma unroll
    for (int j = 0; j < 4; ++j) {
        u32 d = pk_bf16(bf_lo(v[j]) - mb, bf_hi(v[j]) - mb);
        r[j] = pk_bf16(bf_lo(d) * rs, bf_hi(d) * rs);
    }
    return r;
}

__device__ __forceinline__ bf16x8 colfrag(const u32x4* in, int c) {
    const u32 sel = (c & 1) ? 0x07060302u : 0x05040100u;
    u32x4 r;
#pragma unroll
    for (int q = 0; q < 4; ++q) r[q] = __builtin_amdgcn_perm(in[2 * q + 1][c >> 1], in[2 * q][c >> 1], sel);
    return __builtin_bit_cast(bf16x8, r);
}
__device__ __forceinline__ float dot2(u32 a, u32 b, float c) {
    return __builtin_amdgcn_fdot2_f32_bf16(__builtin_bit_cast(bf16x2, a), __builtin_bit_cast(bf16x2, b), c, false);
}
__device__ __forceinline__ f32x4 mfma(bf16x8 a, bf16x8 b, f32x4 c) { return __builtin_amdgcn_mfma_f32_16x16x32_bf16(a, b, c, 0, 0, 0); }
__device__ __forceinline__ bf16x8 asfrag(u32x4 v) { return __builtin_bit_cast(bf16x8, v); }

#define TIMING
#define STAMP(k) do { u64 t_; asm volatile("s_memrealtime %0\n\ts_waitcnt lgkmcnt(0)" : "=s"(t_) :: "memory"); ts_[k] = t_; } while (0)
#define STAMP_INIT u64 ts_[13] = {0, 0, 0, 0, 0, 0, 0, 0, 0, 0, 0, 0, 0}
#define STAMP_FLUSH do { if (l == 0 && (wg == 0 || wg == 40) && (w == 0 || w == 15 || (wg == 40 && w == 4)) && ((ts_[0] >> 9) & 7) == 0) { printf("T wg%d w%d t0 %u: %d %d %d %d %d %d %d %d %d %d %d %d\n", wg, w, (u32)ts_[0], (int)(ts_[1]-ts_[0]), (int)(ts_[2]-ts_[0]), (int)(ts_[3]-ts_[0]), (int)(ts_[4]-ts_[0]), (int)(ts_[5]-ts_[0]), (int)(ts_[6]-ts_[0]), (int)(ts_[7]-ts_[0]), (int)(ts_[8]-ts_[0]), (int)(ts_[9]-ts_[0]), (int)(ts_[10]-ts_[0]), (int)(ts_[11]-ts_[0]), (int)(ts_[12]-ts_[0])); } } while (0)

__global__ __launch_bounds__(1024) void fused_block(const u16* __restrict__ x, const u16* __restrict__ kc, const u16* __restrict__ vc,
                                                    const u16* __restrict__ wq, const u16* __restrict__ wo, const u16* __restrict__ wg_,
                                                    const u16* __restrict__ wu, const u16* __restrict__ wd, u16* __restrict__ out,
                                                    char* __restrict__ ws) {
    __shared__ __attribute__((aligned(16))) float s_q[16 * 132];
    __shared__ __attribute__((aligned(16))) float s_o[16 * 132];
    __shared__ __attribute__((aligned(16))) float s_y[4 * 512];
    __shared__ __attribute__((aligned(16))) u16 s_qb[128];
    __shared__ __attribute__((aligned(16))) u16 s_ob[128];
    __shared__ __attribute__((aligned(16))) float s_m[16];
    __shared__ __attribute__((aligned(16))) float s_l[16];
    __shared__ __attribute__((aligned(16))) float s_r[64 * 20];
    __shared__ __attribute__((aligned(16))) float s_r2[64 * 20];
    __shared__ __attribute__((aligned(16))) u16 s_hn[4 * 520];
    __shared__ __attribute__((aligned(16))) float s_gu[2 * 8 * 32 * 4];
    __shared__ __attribute__((aligned(16))) u16 s_a[4 * 32];

    const int t = threadIdx.x, l = t & 63, g = l >> 4, n = l & 15;
    const int w = __builtin_amdgcn_readfirstlane(t >> 6);
    const int wg = blockIdx.x;
#define HMAP 0
    const int h = HMAP == 0 ? (wg & 7) * 4 + (wg >> 6) : HMAP == 1 ? (wg & 7) + (wg >> 6) * 8 : (wg >> 3), jj = HMAP == 2 ? (wg & 7) : (wg >> 3) & 7, b = jj >> 1, half = jj & 1;
    const int bh = b * H + h, ys = bh * 2 + half;
    const bool is_red = wg < NRED, is_mlp = wg >= NRED && wg < NRED + NMLP;
    const int mj = wg - NRED;

    const rsrc_t r_ws = mk_rsrc(ws, WS_USED);
    const rsrc_t r_x = mk_rsrc(x, NB * D * 2);
    STAMP_INIT;
    STAMP(0);

    u32x2 ev = {0u, 0u};
    if (w == 15) {
        ev = __builtin_amdgcn_raw_buffer_load_b64(r_ws, OFF_E, 0, AUX_SC1);
        const u32x4 s4 = {SENT, SENT, SENT, SENT};
        st16_sc1(s4, r_ws, OFF_Y + ys * 2048 + l * 16);
        st16_sc1(s4, r_ws, OFF_Y + ys * 2048 + 1024 + l * 16);
        if (l == 0) st8_sc1(u32x2{SENT, SENT}, r_ws, OFF_ML + ys * 8);
        if (is_red && l < 8) st16_sc1(s4, r_ws, OFF_H + wg * 128 + l * 16);
        if (is_mlp) {
#pragma unroll
            for (int i = 0; i < 8; ++i) st16_sc1(s4, r_ws, OFF_D + mj * 8192 + i * 1024 + l * 16);
        }
    }
    pin();

    const u32x4 xrow = ld16(r_x, b * 1024 + l * 16, 0, 0);
    const u32x4 xfr = ld16(r_x, b * 1024 + (4 * w + g) * 16, 0, 0);
    u32x4 wv[8], kv[8], vv[8];
    {
        const rsrc_t r = mk_rsrc(wq + h * DH, (D * H * DH - h * DH) * 2);
        const u32 o = (u32)(32 * w + g * 8) * 8192u + n * 16;
#pragma unroll
        for (int i = 0; i < 8; ++i) wv[i] = ldx<WQ_AUX>(r, o, i * 8192);
    }
    pin();
    asm volatile("s_barrier" ::: "memory");
    pin();
    const size_t kvbase = ((size_t)bh * SK + (SK - W) + half * 512) * DH;
    const u32 kvo = w * 8192 + l * 16;
    {
        const rsrc_t r = mk_rsrc(kc + kvbase, 512 * DH * 2);
#pragma unroll
        for (int i = 0; i < 8; ++i) kv[i] = ld16(r, kvo + (i & 3) * 1024, (i >> 2) * 4096, 1);
    }
    pin();

    {
        float mb, rs;
        ln_stats(xrow, mb, rs);
        const bf16x8 xa = asfrag(ln_apply(xfr, mb, rs));
        STAMP(1);
        float qp[8];
#pragma unroll
        for (int c = 0; c < 8; ++c) qp[c] = mfma(xa, colfrag(wv, c), f32x4{0.f, 0.f, 0.f, 0.f})[0];
        if (l < 16) {
            *(f32x4*)&s_q[w * 132 + n * 8] = f32x4{qp[0], qp[1], qp[2], qp[3]};
            *(f32x4*)&s_q[w * 132 + n * 8 + 4] = f32x4{qp[4], qp[5], qp[6], qp[7]};
        }
    }
    pin();
    STAMP(2);

    u32 tg0 = 0, tg1 = 0;
    if (w == 15) {
        asm volatile("s_waitcnt vmcnt(8)" ::: "memory");
        const u64 e = ((u64)(u32)__builtin_amdgcn_readfirstlane(ev[1]) << 32) | (u32)__builtin_amdgcn_readfirstlane(ev[0]);
        const u64 tag = e * 6364136223846793005ull + 1442695040888963407ull;
        tg0 = (u32)tag; tg1 = (u32)(tag >> 32);
        if (l == 0) st8_sc1(u32x2{tg0, tg1}, r_ws, OFF_FLAG + wg * 8);
    }
    pin();

    const int cg = w & 3, kq = w >> 2;
    {
        const rsrc_t r = mk_rsrc(wo + (size_t)h * DH * D, DH * D * 2);
        const u32 o = (u32)(kq * 32 + g * 8) * 1024u + cg * 256 + n * 16;
#pragma unroll
        for (int i = 0; i < 8; ++i) wv[i] = ldx<WO_AUX>(r, o + (i & 3) * 1024, (i >> 2) * 4096);
    }
    {
        const rsrc_t r = mk_rsrc(vc + kvbase, 512 * DH * 2);
#pragma unroll
        for (int i = 0; i < 8; ++i) vv[i] = ld16(r, kvo + (i & 3) * 1024, (i >> 2) * 4096, 1);
    }
    u32x4 fl0 = {0, 0, 0, 0}, fl1 = {0, 0, 0, 0};
    const bool chk = (w == 15) && (is_red || is_mlp);
    if (chk) { fl0 = ld16_sc1(r_ws, OFF_FLAG + l * 16); fl1 = ld16_sc1(r_ws, OFF_FLAG + 1024 + l * 16); }
    pin();

    lds_barrier();
    {
        const int d = t >> 3, s = t & 7;
        float v = s_q[(2 * s) * 132 + d] + s_q[(2 * s + 1) * 132 + d];
        v = row8_sum(v);
        if (s == 0) s_qb[d] = (u16)pk_bf16(v, v);
    }
    lds_barrier();
    const u32x4 qf = *(const u32x4*)&s_qb[n * 8];
    STAMP(3);

    float sc[8];
#pragma unroll
    for (int i = 0; i < 8; ++i) {
        float a = 0.f;
#pragma unroll
        for (int j = 0; j < 4; ++j)
            a = dot2(kv[i][j], qf[j], a);
        sc[i] = row16_sum(a);
    }
#pragma unroll
    for (int i = 0; i < 8; i += 2) {
        u32 p = pk_bf16(sc[i], sc[i + 1]);
        p = pk_bf16(bf_lo(p) * 0.08838834764831845f, bf_hi(p) * 0.08838834764831845f);
        sc[i] = bf_lo(p); sc[i + 1] = bf_hi(p);
    }
    float mx = fmaxf(fmaxf(fmaxf(sc[0], sc[1]), fmaxf(sc[2], sc[3])), fmaxf(fmaxf(sc[4], sc[5]), fmaxf(sc[6], sc[7])));
    mx = xrow_max(mx);
    if (l == 0) s_m[w] = mx;
    STAMP(4);
    lds_barrier();
    float M;
    {
        const f32x4 a = *(const f32x4*)&s_m[0], bb = *(const f32x4*)&s_m[4], c = *(const f32x4*)&s_m[8], d = *(const f32x4*)&s_m[12];
        M = fmaxf(fmaxf(fmaxf(fmaxf(a[0], a[1]), fmaxf(a[2], a[3])), fmaxf(fmaxf(bb[0], bb[1]), fmaxf(bb[2], bb[3]))),
                  fmaxf(fmaxf(fmaxf(c[0], c[1]), fmaxf(c[2], c[3])), fmaxf(fmaxf(d[0], d[1]), fmaxf(d[2], d[3]))));
    }
    u32x4 pa;
    {
        float ls = 0.f;
#pragma unroll
        for (int i = 0; i < 8; i += 2) {
            const float p0 = __builtin_amdgcn_exp2f((sc[i] - M) * 1.4426950408889634f);
            const float p1 = __builtin_amdgcn_exp2f((sc[i + 1] - M) * 1.4426950408889634f);
            ls += p0 + p1;
            pa[i >> 1] = pk_bf16(p0, p1);
        }
        ls = xrow_sum(ls);
        if (l == 0) s_l[w] = ls;
    }
    bf16x8 wf[8];
#pragma unroll
    for (int c = 0; c < 8; ++c) wf[c] = colfrag(wv, c);
    pin();
    STAMP(5);

    {
        float op[8];
#pragma unroll
        for (int c = 0; c < 8; ++c) op[c] = mfma(asfrag(pa), colfrag(vv, c), f32x4{0.f, 0.f, 0.f, 0.f})[0];
        if (l < 16) {
            *(f32x4*)&s_o[w * 132 + n * 8] = f32x4{op[0], op[1], op[2], op[3]};
            *(f32x4*)&s_o[w * 132 + n * 8 + 4] = f32x4{op[4], op[5], op[6], op[7]};
        }
    }
    STAMP(6);
    if (chk) {
        for (u32 spin = 0; spin < SPIN_CAP; ++spin) {
            const bool ok = fl0[0] == tg0 && fl0[1] == tg1 && fl0[2] == tg0 && fl0[3] == tg1 &&
                            fl1[0] == tg0 && fl1[1] == tg1 && fl1[2] == tg0 && fl1[3] == tg1;
            if (__all(ok)) break;
            asm volatile("" ::: "memory");
            fl0 = ld16_sc1(r_ws, OFF_FLAG + l * 16); fl1 = ld16_sc1(r_ws, OFF_FLAG + 1024 + l * 16);
        }
        if (wg == 0 && l == 0) st8_sc1(u32x2{tg0, tg1}, r_ws, OFF_E);
    }
    lds_barrier();
    float L;
    {
        const f32x4 a = *(const f32x4*)&s_l[0], bb = *(const f32x4*)&s_l[4], c = *(const f32x4*)&s_l[8], d = *(const f32x4*)&s_l[12];
        const f32x4 e = (a + bb) + (c + d);
        L = (e[0] + e[1]) + (e[2] + e[3]);
        const int d0 = t >> 3, s = t & 7;
        float v = s_o[(2 * s) * 132 + d0] + s_o[(2 * s + 1) * 132 + d0];
        v = row8_sum(v) * __builtin_amdgcn_rcpf(L);
        if (s == 0) s_ob[d0] = (u16)pk_bf16(v, v);
    }
    lds_barrier();
    {
        const bf16x8 oa = asfrag(*(const u32x4*)&s_ob[kq * 32 + g * 8]);
        float yp[8];
#pragma unroll
        for (int c = 0; c < 8; ++c) yp[c] = mfma(oa, wf[c], f32x4{0.f, 0.f, 0.f, 0.f})[0];
        if (l < 16) {
            *(f32x4*)&s_y[kq * 512 + cg * 128 + n * 8] = f32x4{yp[0], yp[1], yp[2], yp[3]};
            *(f32x4*)&s_y[kq * 512 + cg * 128 + n * 8 + 4] = f32x4{yp[4], yp[5], yp[6], yp[7]};
        }
    }
    lds_barrier();
    if (w < 2) {
        const f32x4 a = *(const f32x4*)&s_y[t * 4], bb = *(const f32x4*)&s_y[512 + t * 4];
        const f32x4 c = *(const f32x4*)&s_y[1024 + t * 4], d = *(const f32x4*)&s_y[1536 + t * 4];
        st16_sc1(canon4((a + bb) + (c + d)), r_ws, OFF_Y + ys * 2048 + t * 16);
        if (t == 0) st8_sc1(u32x2{canon(__float_as_uint(M)), canon(__float_as_uint(L))}, r_ws, OFF_ML + ys * 8);
    }
    pin();
    STAMP(7);

    if (is_red) {
        const int rb = wg >> 3, ch = wg & 7;
        float xres = 0.f;
        if (w == 0) xres = __uint_as_float((u32)x[rb * D + ch * 64 + l] << 16);
        const int p = w * 4 + g;
        const u32 yo = OFF_Y + (u32)(rb * 64 + p) * 2048u + ch * 256 + n * 16;
        const u32 mo = OFF_ML + (u32)(rb * 32 + (p >> 1)) * 16u;
        u32x4 yv, ml;
        for (u32 spin = 0; spin < SPIN_CAP; ++spin) {
            asm volatile("" ::: "memory");
            yv = ld16_sc1(r_ws, yo);
            ml = ld16_sc1(r_ws, mo);
            if (__all(full4(yv) && full4(ml))) break;
        }
        STAMP(8);
        {
            const float m0 = __uint_as_float(ml[0]), l0 = __uint_as_float(ml[1]), m1 = __uint_as_float(ml[2]), l1 = __uint_as_float(ml[3]);
            const float mm = fmaxf(m0, m1);
            const float e0 = l0 * __builtin_amdgcn_exp2f((m0 - mm) * 1.4426950408889634f);
            const float e1 = l1 * __builtin_amdgcn_exp2f((m1 - mm) * 1.4426950408889634f);
            const float wt = ((p & 1) ? e1 : e0) * __builtin_amdgcn_rcpf(e0 + e1);
            float v[4];
#pragma unroll
            for (int i = 0; i < 4; ++i) v[i] = xrow_sum(__uint_as_float(yv[i]) * wt);
            if (l < 16) {
#pragma unroll
                for (int i = 0; i < 4; ++i) s_r[(n * 4 + i) * 20 + w] = v[i];
            }
        }
        lds_barrier();
        float hval = 0.f;
        if (w == 0) {
            const f32x4 a = *(const f32x4*)&s_r[l * 20], bb = *(const f32x4*)&s_r[l * 20 + 4];
            const f32x4 c = *(const f32x4*)&s_r[l * 20 + 8], d = *(const f32x4*)&s_r[l * 20 + 12];
            const f32x4 e = (a + bb) + (c + d);
            hval = bfr(xres + bfr((e[0] + e[1]) + (e[2] + e[3])));
            const u32 pr = __builtin_amdgcn_perm(dppu<DPP_SHL1>(__float_as_uint(hval)), __float_as_uint(hval), 0x07060302u);
            const u32x4 hv = {canon(pr), canon(dppu<DPP_SHL2>(pr)), canon(dppu<DPP_SHL4>(pr)), canon(dppu<DPP_SHL6>(pr))};
            if ((l & 7) == 0) st16_sc1(hv, r_ws, OFF_H + wg * 128 + l * 2);
        }
        STAMP(9);
        if (w < 11) {
            const int pj = w * 4 + g;
            const int pjc = pj < NMLP ? pj : NMLP - 1;
            const u32 o = OFF_D + (u32)pjc * 8192u + (ch >> 1) * 2048 + (n >> 3) * 1024 + rb * 256 + (ch & 1) * 128 + (n & 7) * 16;
            u32x4 dv;
            for (u32 spin = 0; spin < SPIN_CAP; ++spin) {
                asm volatile("" ::: "memory");
                dv = ld16_sc1(r_ws, o);
                if (__all(full4(dv))) break;
            }
            STAMP(10);
            float v[4];
#pragma unroll
            for (int i = 0; i < 4; ++i) v[i] = xrow_sum(pj < NMLP ? __uint_as_float(dv[i]) : 0.f);
            if (l < 16) {
#pragma unroll
                for (int i = 0; i < 4; ++i) s_r2[((n & 7) * 8 + (n >> 3) * 4 + i) * 20 + w] = v[i];
            }
        }
        lds_barrier();
        if (w == 0) {
            const f32x4 a = *(const f32x4*)&s_r2[l * 20], bb = *(const f32x4*)&s_r2[l * 20 + 4], c = *(const f32x4*)&s_r2[l * 20 + 8];
            const f32x4 e = a + bb;
            const float sum = ((e[0] + e[1]) + (e[2] + e[3])) + ((c[0] + c[1]) + c[2]);
            const float o = hval + bfr(sum);
            out[rb * D + ch * 64 + l] = (u16)pk_bf16(o, o);
        }
        STAMP(11);
    } else if (is_mlp) {
        if (w < 4) {
            const int mat = w & 1, kh = w >> 1, kb = n >> 2, chunk = n & 3;
            const rsrc_t r = mk_rsrc(mat ? wu : wg_, D * F * 2);
            const u32 o = (u32)(kh * 256 + kb * 32 + g * 8) * (F * 2) + mj * 64 + chunk * 16;
            u32x4 w0[8], w1[8];
#pragma unroll
            for (int i = 0; i < 8; ++i) w0[i] = ld16(r, o, i * (F * 2), 1);
#pragma unroll
            for (int i = 0; i < 8; ++i) w1[i] = ld16(r, o, (128 + i) * (F * 2), 1);
            bf16x8 f0[8], f1[8];
#pragma unroll
            for (int c = 0; c < 8; ++c) { f0[c] = colfrag(w0, c); }
#pragma unroll
            for (int c = 0; c < 8; ++c) { f1[c] = colfrag(w1, c); }
            pin();
            lds_barrier();
            const int ab = n & 3, akb = n >> 2;
            const bf16x8 a0 = asfrag(*(const u32x4*)&s_hn[ab * 520 + kh * 256 + akb * 32 + g * 8]);
            const bf16x8 a1 = asfrag(*(const u32x4*)&s_hn[ab * 520 + kh * 256 + 128 + akb * 32 + g * 8]);
            f32x4 acc[8];
#pragma unroll
            for (int c = 0; c < 8; ++c) acc[c] = mfma(a0, f0[c], f32x4{0.f, 0.f, 0.f, 0.f});
#pragma unroll
            for (int c = 0; c < 8; ++c) acc[c] = mfma(a1, f1[c], acc[c]);
            if (kb == g) {
#pragma unroll
                for (int c = 0; c < 8; ++c) *(f32x4*)&s_gu[((mat * 8 + kh * 4 + kb) * 32 + chunk * 8 + c) * 4] = acc[c];
            }
            lds_barrier();
            if (w == 0) {
                f32x4 s = {0.f, 0.f, 0.f, 0.f};
                const int m2 = l >> 5, col = l & 31;
#pragma unroll
                for (int i = 0; i < 8; ++i) s += *(const f32x4*)&s_gu[((m2 * 8 + i) * 32 + col) * 4];
#pragma unroll
                for (int i = 0; i < 4; ++i) {
                    const float v = bfr(s[i]);
                    auto sw = __builtin_amdgcn_permlane32_swap(__float_as_uint(v), __float_as_uint(v), false, false);
                    const float pr = __uint_as_float(sw[0]) * __uint_as_float(sw[1]);
                    if (l < 32) s_a[i * 32 + col] = (u16)pk_bf16(pr, pr);
                }
            }
            lds_barrier();
        } else if (w < 8) {
            const int c4 = w - 4;
            const rsrc_t r = mk_rsrc(wd + (size_t)mj * 32 * D, 32 * D * 2);
            const u32 o = (u32)(g * 8) * 1024u + c4 * 256 + n * 16;
            u32x4 w0[8];
#pragma unroll
            for (int i = 0; i < 8; ++i) w0[i] = ld16(r, o + (i & 3) * 1024, (i >> 2) * 4096, 1);
            bf16x8 f0[8];
#pragma unroll
            for (int c = 0; c < 8; ++c) f0[c] = colfrag(w0, c);
            pin();
            lds_barrier();
            lds_barrier();
            lds_barrier();
            const bf16x8 aa = asfrag(*(const u32x4*)&s_a[(n >> 2) * 32 + g * 8]);
            float dp[8];
#pragma unroll
            for (int c = 0; c < 8; ++c) dp[c] = mfma(aa, f0[c], f32x4{0.f, 0.f, 0.f, 0.f})[0];
            const u32 so = OFF_D + (u32)mj * 8192u + c4 * 2048 + l * 16;
            st16_sc1(canon4(f32x4{dp[0], dp[1], dp[2], dp[3]}), r_ws, so);
            st16_sc1(canon4(f32x4{dp[4], dp[5], dp[6], dp[7]}), r_ws, so + 1024);
            STAMP(11);
        } else if (w >= 12) {
            const int hb = w - 12;
            u32x4 hv;
            for (u32 spin = 0; spin < SPIN_CAP; ++spin) {
                asm volatile("" ::: "memory");
                hv = ld16_sc1(r_ws, OFF_H + hb * 1024 + l * 16);
                if (__all(full4(hv))) break;
            }
            STAMP(8);
            float mb, rs;
            ln_stats(hv, mb, rs);
            *(u32x4*)&s_hn[hb * 520 + l * 8] = ln_apply(hv, mb, rs);
            STAMP(9);
            lds_barrier();
            lds_barrier();
            lds_barrier();
        } else {
            lds_barrier();
            lds_barrier();
            lds_barrier();
        }
    }
    STAMP(12);
    STAMP_FLUSH;
}

extern "C" void kernel_launch(void* const* d_in, const int* in_sizes, int n_in, void* d_out, int out_size, void* d_ws,
                              size_t ws_size, hipStream_t stream) {
    fused_block<<<NWG, 1024, 0, stream>>>((const u16*)d_in[0], (const u16*)d_in[1], (const u16*)d_in[2], (const u16*)d_in[3],
                                          (const u16*)d_in[4], (const u16*)d_in[6], (const u16*)d_in[7], (const u16*)d_in[8],
                                          (u16*)d_out, (char*)d_ws);
}
```

```cpp
#define PADWG 0
#define PADW 0
#define PADK 0
#include <hip/hip_runtime.h>
#include <stdint.h>

typedef unsigned short u16;
typedef unsigned int u32;
typedef unsigned long long u64;
typedef __attribute__((ext_vector_type(2))) u32 u32x2;
typedef __attribute__((ext_vector_type(4))) u32 u32x4;
typedef __attribute__((ext_vector_type(2))) float f32x2;
typedef __attribute__((ext_vector_type(4))) float f32x4;
typedef __attribute__((ext_vector_type(2))) __bf16 bf16x2;
typedef __attribute__((ext_vector_type(8))) __bf16 bf16x8;
typedef __amdgpu_buffer_rsrc_t rsrc_t;

constexpr int NB = 4, D = 512, H = 32, DH = 128, SK = 4096, W = 1024, F = 1376;
constexpr int NWG = 256, NRED = 32, NMLP = 43;

constexpr u32 OFF_E = 0;
constexpr u32 OFF_FLAG = 1024;
constexpr u32 OFF_ML = 4096;
constexpr u32 OFF_H = 8192;
constexpr u32 OFF_Y = 16384;
constexpr u32 OFF_D = OFF_Y + NWG * 2048;
constexpr u32 WS_USED = OFF_D + NMLP * 8192;
constexpr u32 OFF_TS = 1u << 20;

constexpr u32 SENT = 0xFFFFFFFFu;
constexpr int AUX_NT = 2, AUX_SC1 = 16;
constexpr u32 SPIN_CAP = 1u << 18;

__device__ __forceinline__ rsrc_t mk_rsrc(const void* p, u32 bytes) {
    return __builtin_amdgcn_make_buffer_rsrc((void*)p, 0, (int)bytes, 0x00020000);
}
#define WQ_AUX 2
#define WO_AUX 2
template <int AUX> __device__ __forceinline__ u32x4 ldx(rsrc_t r, u32 voff, u32 soff) { return __builtin_amdgcn_raw_buffer_load_b128(r, voff, soff, AUX); }
__device__ __forceinline__ u32x4 ld16(rsrc_t r, u32 voff, u32 soff, const int aux_nt) {
    return aux_nt ? __builtin_amdgcn_raw_buffer_load_b128(r, voff, soff, AUX_NT)
                  : __builtin_amdgcn_raw_buffer_load_b128(r, voff, soff, 0);
}
__device__ __forceinline__ u32x4 ld16_sc1(rsrc_t r, u32 voff) { return __builtin_amdgcn_raw_buffer_load_b128(r, voff, 0, AUX_SC1); }
__device__ __forceinline__ void st16_sc1(u32x4 v, rsrc_t r, u32 voff) { __builtin_amdgcn_raw_buffer_store_b128(v, r, voff, 0, AUX_SC1); }
__device__ __forceinline__ void st8_sc1(u32x2 v, rsrc_t r, u32 voff) { __builtin_amdgcn_raw_buffer_store_b64(v, r, voff, 0, AUX_SC1); }

__device__ __forceinline__ float bf_lo(u32 v) { return __uint_as_float(v << 16); }
__device__ __forceinline__ float bf_hi(u32 v) { return __uint_as_float(v & 0xffff0000u); }
__device__ __forceinline__ u32 pk_bf16(float a, float b) {
    f32x2 v = {a, b};
    return __builtin_bit_cast(u32, __builtin_convertvector(v, bf16x2));
}
__device__ __forceinline__ float bfr(float a) { return bf_lo(pk_bf16(a, a)); }
__device__ __forceinline__ u32 canon(u32 v) { return v < SENT ? v : SENT - 1; }
__device__ __forceinline__ u32x4 canon4(f32x4 v) {
    u32x4 r;
#pragma unroll
    for (int i = 0; i < 4; ++i) r[i] = canon(__float_as_uint(v[i]));
    return r;
}
__device__ __forceinline__ bool full4(u32x4 v) { return v[0] != SENT && v[1] != SENT && v[2] != SENT && v[3] != SENT; }

template <int CTRL> __device__ __forceinline__ float dpp(float v) {
    return __uint_as_float(__builtin_amdgcn_update_dpp(0u, __float_as_uint(v), CTRL, 0xf, 0xf, true));
}
template <int CTRL> __device__ __forceinline__ u32 dppu(u32 v) { return __builtin_amdgcn_update_dpp(0u, v, CTRL, 0xf, 0xf, true); }
constexpr int DPP_X1 = 0xB1, DPP_X2 = 0x4E, DPP_HMIR = 0x141, DPP_MIR = 0x140;
constexpr int DPP_SHL1 = 0x101, DPP_SHL2 = 0x102, DPP_SHL4 = 0x104, DPP_SHL6 = 0x106;

__device__ __forceinline__ float row8_sum(float v) { v += dpp<DPP_X1>(v); v += dpp<DPP_X2>(v); v += dpp<DPP_HMIR>(v); return v; }
__device__ __forceinline__ float row16_sum(float v) { v = row8_sum(v); v += dpp<DPP_MIR>(v); return v; }
__device__ __forceinline__ float xrow_sum(float x) {
    auto s = __builtin_amdgcn_permlane16_swap(__float_as_uint(x), __float_as_uint(x), false, false);
    x = __uint_as_float(s[0]) + __uint_as_float(s[1]);
    auto t = __builtin_amdgcn_permlane32_swap(__float_as_uint(x), __float_as_uint(x), false, false);
    return __uint_as_float(t[0]) + __uint_as_float(t[1]);
}
__device__ __forceinline__ float xrow_max(float x) {
    auto s = __builtin_amdgcn_permlane16_swap(__float_as_uint(x), __float_as_uint(x), false, false);
    x = fmaxf(__uint_as_float(s[0]), __uint_as_float(s[1]));
    auto t = __builtin_amdgcn_permlane32_swap(__float_as_uint(x), __float_as_uint(x), false, false);
    return fmaxf(__uint_as_float(t[0]), __uint_as_float(t[1]));
}
__device__ __forceinline__ float wave_sum(float v) { return xrow_sum(row16_sum(v)); }

__device__ __forceinline__ void lds_barrier() { asm volatile("s_waitcnt lgkmcnt(0)\n\ts_barrier" ::: "memory"); }
__device__ __forceinline__ void pin() { __builtin_amdgcn_sched_barrier(0); }

__device__ __forceinline__ void unpack8(u32x4 v, float* f) {
#pragma unroll
    for (int j = 0; j < 4; ++j) { f[2 * j] = bf_lo(v[j]); f[2 * j + 1] = bf_hi(v[j]); }
}

__device__ __forceinline__ void ln_stats(u32x4 row, float& mb, float& rs) {
    float f[8];
    unpack8(row, f);
    float s = ((f[0] + f[1]) + (f[2] + f[3])) + ((f[4] + f[5]) + (f[6] + f[7]));
    float mean = wave_sum(s) * (1.f / D);
    float q = 0.f;
#pragma unroll
    for (int i = 0; i < 8; ++i) { float d = f[i] - mean; q += d * d; }
    float var = wave_sum(q) * (1.f / D);
    mb = bfr(mean);
    float sq = bfr(__builtin_amdgcn_sqrtf(bfr(bfr(var) + 1e-5f)));
    rs = __builtin_amdgcn_rcpf(sq);
}
__device__ __forceinline__ u32x4 ln_apply(u32x4 v, float mb, float rs) {
    u32x4 r;
#pragma unroll
    for (int j = 0; j < 4; ++j) {
        u32 d = pk_bf16(bf_lo(v[j]) - mb, bf_hi(v[j]) - mb);
        r[j] = pk_bf16(bf_lo(d) * rs, bf_hi(d) * rs);
    }
    return r;
}

__device__ __forceinline__ bf16x8 colfrag(const u32x4* in, int c) {
    const u32 sel = (c & 1) ? 0x07060302u : 0x05040100u;
    u32x4 r;
#pragma unroll
    for (int q = 0; q < 4; ++q) r[q] = __builtin_amdgcn_perm(in[2 * q + 1][c >> 1], in[2 * q][c >> 1], sel);
    return __builtin_bit_cast(bf16x8, r);
}
__device__ __forceinline__ float dot2(u32 a, u32 b, float c) {
    return __builtin_amdgcn_fdot2_f32_bf16(__builtin_bit_cast(bf16x2, a), __builtin_bit_cast(bf16x2, b), c, false);
}
__device__ __forceinline__ f32x4 mfma(bf16x8 a, bf16x8 b, f32x4 c) { return __builtin_amdgcn_mfma_f32_16x16x32_bf16(a, b, c, 0, 0, 0); }
__device__ __forceinline__ bf16x8 asfrag(u32x4 v) { return __builtin_bit_cast(bf16x8, v); }

#define TIMING
#define STAMP(k) do { u64 t_; asm volatile("s_memrealtime %0\n\ts_waitcnt lgkmcnt(0)" : "=s"(t_) :: "memory"); ts_[k] = t_; } while (0)
#define STAMP_INIT u64 ts_[13] = {0, 0, 0, 0, 0, 0, 0, 0, 0, 0, 0, 0, 0}
#define STAMP_FLUSH do { if (wg == PADWG && w == PADW) { const u64 tgt_ = ts_[PADK] + 2600; for (u32 it_ = 0; it_ < 100000; ++it_) { u64 t_; asm volatile("s_memrealtime %0\n\ts_waitcnt lgkmcnt(0)" : "=s"(t_) :: "memory"); if (t_ >= tgt_) break; } } } while (0)

__global__ __launch_bounds__(1024) void fused_block(const u16* __restrict__ x, const u16* __restrict__ kc, const u16* __restrict__ vc,
                                                    const u16* __restrict__ wq, const u16* __restrict__ wo, const u16* __restrict__ wg_,
                                                    const u16* __restrict__ wu, const u16* __restrict__ wd, u16* __restrict__ out,
                                                    char* __restrict__ ws) {
    STAMP_INIT;
    STAMP(0);
    __builtin_amdgcn_sched_barrier(0);
    __shared__ __attribute__((aligned(16))) float s_q[16 * 132];
    __shared__ __attribute__((aligned(16))) float s_o[16 * 132];
    __shared__ __attribute__((aligned(16))) float s_y[4 * 512];
    __shared__ __attribute__((aligned(16))) u16 s_qb[128];
    __shared__ __attribute__((aligned(16))) u16 s_ob[128];
    __shared__ __attribute__((aligned(16))) float s_m[16];
    __shared__ __attribute__((aligned(16))) float s_l[16];
    __shared__ __attribute__((aligned(16))) float s_r[64 * 20];
    __shared__ __attribute__((aligned(16))) float s_r2[64 * 20];
    __shared__ __attribute__((aligned(16))) u16 s_hn[4 * 520];
    __shared__ __attribute__((aligned(16))) float s_gu[2 * 8 * 32 * 4];
    __shared__ __attribute__((aligned(16))) u16 s_a[4 * 32];

    const int t = threadIdx.x, l = t & 63, g = l >> 4, n = l & 15;
    const int w = __builtin_amdgcn_readfirstlane(t >> 6);
    const int wg = blockIdx.x;
#define HMAP 0
    const int h = HMAP == 0 ? (wg & 7) * 4 + (wg >> 6) : HMAP == 1 ? (wg & 7) + (wg >> 6) * 8 : (wg >> 3), jj = HMAP == 2 ? (wg & 7) : (wg >> 3) & 7, b = jj >> 1, half = jj & 1;
    const int bh = b * H + h, ys = bh * 2 + half;
    const bool is_red = wg < NRED, is_mlp = wg >= NRED && wg < NRED + NMLP;
    const int mj = wg - NRED;

    const rsrc_t r_ws = mk_rsrc(ws, WS_USED);
    const rsrc_t r_x = mk_rsrc(x, NB * D * 2);

    u32x2 ev = {0u, 0u};
    if (w == 15) {
        ev = __builtin_amdgcn_raw_buffer_load_b64(r_ws, OFF_E, 0, AUX_SC1);
        const u32x4 s4 = {SENT, SENT, SENT, SENT};
        st16_sc1(s4, r_ws, OFF_Y + ys * 2048 + l * 16);
        st16_sc1(s4, r_ws, OFF_Y + ys * 2048 + 1024 + l * 16);
        if (l == 0) st8_sc1(u32x2{SENT, SENT}, r_ws, OFF_ML + ys * 8);
        if (is_red && l < 8) st16_sc1(s4, r_ws, OFF_H + wg * 128 + l * 16);
        if (is_mlp) {
#pragma unroll
            for (int i = 0; i < 8; ++i) st16_sc1(s4, r_ws, OFF_D + mj * 8192 + i * 1024 + l * 16);
        }
    }
    pin();

    asm volatile("" :: "s"(x), "s"(kc), "s"(vc), "s"(wq), "s"(wo), "s"(wg_), "s"(wu), "s"(wd), "s"(out));
    STAMP(1);
    const u32x4 xrow = ld16(r_x, b * 1024 + l * 16, 0, 0);
    const u32x4 xfr = ld16(r_x, b * 1024 + (4 * w + g) * 16, 0, 0);
    u32x4 wv[8], kv[8], vv[8];
    {
        const rsrc_t r = mk_rsrc(wq + h * DH, (D * H * DH - h * DH) * 2);
        const u32 o = (u32)(32 * w + g * 8) * 8192u + n * 16;
#pragma unroll
        for (int i = 0; i < 8; ++i) wv[i] = ldx<WQ_AUX>(r, o, i * 8192);
    }
    pin();
    asm volatile("s_barrier" ::: "memory");
    pin();
    const size_t kvbase = ((size_t)bh * SK + (SK - W) + half * 512) * DH;
    const u32 kvo = w * 8192 + l * 16;
    {
        const rsrc_t r = mk_rsrc(kc + kvbase, 512 * DH * 2);
#pragma unroll
        for (int i = 0; i < 8; ++i) kv[i] = ld16(r, kvo + (i & 3) * 1024, (i >> 2) * 4096, 1);
    }
    pin();

    {
        float mb, rs;
        ln_stats(xrow, mb, rs);
        const bf16x8 xa = asfrag(ln_apply(xfr, mb, rs));
        float qp[8];
#pragma unroll
        for (int c = 0; c < 8; ++c) qp[c] = mfma(xa, colfrag(wv, c), f32x4{0.f, 0.f, 0.f, 0.f})[0];
        if (l < 16) {
            *(f32x4*)&s_q[w * 132 + n * 8] = f32x4{qp[0], qp[1], qp[2], qp[3]};
            *(f32x4*)&s_q[w * 132 + n * 8 + 4] = f32x4{qp[4], qp[5], qp[6], qp[7]};
        }
    }
    pin();
    STAMP(2);

    u32 tg0 = 0, tg1 = 0;
    if (w == 15) {
        asm volatile("s_waitcnt vmcnt(8)" ::: "memory");
        const u64 e = ((u64)(u32)__builtin_amdgcn_readfirstlane(ev[1]) << 32) | (u32)__builtin_amdgcn_readfirstlane(ev[0]);
        const u64 tag = e * 6364136223846793005ull + 1442695040888963407ull;
        tg0 = (u32)tag; tg1 = (u32)(tag >> 32);
        if (l == 0) st8_sc1(u32x2{tg0, tg1}, r_ws, OFF_FLAG + wg * 8);
    }
    pin();

    const int cg = w & 3, kq = w >> 2;
    {
        const rsrc_t r = mk_rsrc(wo + (size_t)h * DH * D, DH * D * 2);
        const u32 o = (u32)(kq * 32 + g * 8) * 1024u + cg * 256 + n * 16;
#pragma unroll
        for (int i = 0; i < 8; ++i) wv[i] = ldx<WO_AUX>(r, o + (i & 3) * 1024, (i >> 2) * 4096);
    }
    {
        const rsrc_t r = mk_rsrc(vc + kvbase, 512 * DH * 2);
#pragma unroll
        for (int i = 0; i < 8; ++i) vv[i] = ld16(r, kvo + (i & 3) * 1024, (i >> 2) * 4096, 1);
    }
    u32x4 fl0 = {0, 0, 0, 0}, fl1 = {0, 0, 0, 0};
    const bool chk = (w == 15) && (is_red || is_mlp);
    if (chk) { fl0 = ld16_sc1(r_ws, OFF_FLAG + l * 16); fl1 = ld16_sc1(r_ws, OFF_FLAG + 1024 + l * 16); }
    pin();

    lds_barrier();
    {
        const int d = t >> 3, s = t & 7;
        float v = s_q[(2 * s) * 132 + d] + s_q[(2 * s + 1) * 132 + d];
        v = row8_sum(v);
        if (s == 0) s_qb[d] = (u16)pk_bf16(v, v);
    }
    lds_barrier();
    const u32x4 qf = *(const u32x4*)&s_qb[n * 8];
    STAMP(3);

    float sc[8];
#pragma unroll
    for (int i = 0; i < 8; ++i) {
        float a = 0.f;
#pragma unroll
        for (int j = 0; j < 4; ++j)
            a = dot2(kv[i][j], qf[j], a);
        sc[i] = row16_sum(a);
    }
#pragma unroll
    for (int i = 0; i < 8; i += 2) {
        u32 p = pk_bf16(sc[i], sc[i + 1]);
        p = pk_bf16(bf_lo(p) * 0.08838834764831845f, bf_hi(p) * 0.08838834764831845f);
        sc[i] = bf_lo(p); sc[i + 1] = bf_hi(p);
    }
    float mx = fmaxf(fmaxf(fmaxf(sc[0], sc[1]), fmaxf(sc[2], sc[3])), fmaxf(fmaxf(sc[4], sc[5]), fmaxf(sc[6], sc[7])));
    mx = xrow_max(mx);
    if (l == 0) s_m[w] = mx;
    STAMP(4);
    lds_barrier();
    float M;
    {
        const f32x4 a = *(const f32x4*)&s_m[0], bb = *(const f32x4*)&s_m[4], c = *(const f32x4*)&s_m[8], d = *(const f32x4*)&s_m[12];
        M = fmaxf(fmaxf(fmaxf(fmaxf(a[0], a[1]), fmaxf(a[2], a[3])), fmaxf(fmaxf(bb[0], bb[1]), fmaxf(bb[2], bb[3]))),
                  fmaxf(fmaxf(fmaxf(c[0], c[1]), fmaxf(c[2], c[3])), fmaxf(fmaxf(d[0], d[1]), fmaxf(d[2], d[3]))));
    }
    u32x4 pa;
    {
        float ls = 0.f;
#pragma unroll
        for (int i = 0; i < 8; i += 2) {
            const float p0 = __builtin_amdgcn_exp2f((sc[i] - M) * 1.4426950408889634f);
            const float p1 = __builtin_amdgcn_exp2f((sc[i + 1] - M) * 1.4426950408889634f);
            ls += p0 + p1;
            pa[i >> 1] = pk_bf16(p0, p1);
        }
        ls = xrow_sum(ls);
        if (l == 0) s_l[w] = ls;
    }
    bf16x8 wf[8];
#pragma unroll
    for (int c = 0; c < 8; ++c) wf[c] = colfrag(wv, c);
    pin();
    STAMP(5);

    {
        float op[8];
#pragma unroll
        for (int c = 0; c < 8; ++c) op[c] = mfma(asfrag(pa), colfrag(vv, c), f32x4{0.f, 0.f, 0.f, 0.f})[0];
        if (l < 16) {
            *(f32x4*)&s_o[w * 132 + n * 8] = f32x4{op[0], op[1], op[2], op[3]};
            *(f32x4*)&s_o[w * 132 + n * 8 + 4] = f32x4{op[4], op[5], op[6], op[7]};
        }
    }
    STAMP(6);
    if (chk) {
        for (u32 spin = 0; spin < SPIN_CAP; ++spin) {
            const bool ok = fl0[0] == tg0 && fl0[1] == tg1 && fl0[2] == tg0 && fl0[3] == tg1 &&
                            fl1[0] == tg0 && fl1[1] == tg1 && fl1[2] == tg0 && fl1[3] == tg1;
            if (__all(ok)) break;
            asm volatile("" ::: "memory");
            fl0 = ld16_sc1(r_ws, OFF_FLAG + l * 16); fl1 = ld16_sc1(r_ws, OFF_FLAG + 1024 + l * 16);
        }
        if (wg == 0 && l == 0) st8_sc1(u32x2{tg0, tg1}, r_ws, OFF_E);
    }
    lds_barrier();
    float L;
    {
        const f32x4 a = *(const f32x4*)&s_l[0], bb = *(const f32x4*)&s_l[4], c = *(const f32x4*)&s_l[8], d = *(const f32x4*)&s_l[12];
        const f32x4 e = (a + bb) + (c + d);
        L = (e[0] + e[1]) + (e[2] + e[3]);
        const int d0 = t >> 3, s = t & 7;
        float v = s_o[(2 * s) * 132 + d0] + s_o[(2 * s + 1) * 132 + d0];
        v = row8_sum(v) * __builtin_amdgcn_rcpf(L);
        if (s == 0) s_ob[d0] = (u16)pk_bf16(v, v);
    }
    lds_barrier();
    {
        const bf16x8 oa = asfrag(*(const u32x4*)&s_ob[kq * 32 + g * 8]);
        float yp[8];
#pragma unroll
        for (int c = 0; c < 8; ++c) yp[c] = mfma(oa, wf[c], f32x4{0.f, 0.f, 0.f, 0.f})[0];
        if (l < 16) {
            *(f32x4*)&s_y[kq * 512 + cg * 128 + n * 8] = f32x4{yp[0], yp[1], yp[2], yp[3]};
            *(f32x4*)&s_y[kq * 512 + cg * 128 + n * 8 + 4] = f32x4{yp[4], yp[5], yp[6], yp[7]};
        }
    }
    lds_barrier();
    if (w < 2) {
        const f32x4 a = *(const f32x4*)&s_y[t * 4], bb = *(const f32x4*)&s_y[512 + t * 4];
        const f32x4 c = *(const f32x4*)&s_y[1024 + t * 4], d = *(const f32x4*)&s_y[1536 + t * 4];
        st16_sc1(canon4((a + bb) + (c + d)), r_ws, OFF_Y + ys * 2048 + t * 16);
        if (t == 0) st8_sc1(u32x2{canon(__float_as_uint(M)), canon(__float_as_uint(L))}, r_ws, OFF_ML + ys * 8);
    }
    pin();
    STAMP(7);

    if (is_red) {
        const int rb = wg >> 3, ch = wg & 7;
        float xres = 0.f;
        if (w == 0) xres = __uint_as_float((u32)x[rb * D + ch * 64 + l] << 16);
        const int p = w * 4 + g;
        const u32 yo = OFF_Y + (u32)(rb * 64 + p) * 2048u + ch * 256 + n * 16;
        const u32 mo = OFF_ML + (u32)(rb * 32 + (p >> 1)) * 16u;
        u32x4 yv, ml;
        for (u32 spin = 0; spin < SPIN_CAP; ++spin) {
            asm volatile("" ::: "memory");
            yv = ld16_sc1(r_ws, yo);
            ml = ld16_sc1(r_ws, mo);
            if (__all(full4(yv) && full4(ml))) break;
        }
        STAMP(8);
        {
            const float m0 = __uint_as_float(ml[0]), l0 = __uint_as_float(ml[1]), m1 = __uint_as_float(ml[2]), l1 = __uint_as_float(ml[3]);
            const float mm = fmaxf(m0, m1);
            const float e0 = l0 * __builtin_amdgcn_exp2f((m0 - mm) * 1.4426950408889634f);
            const float e1 = l1 * __builtin_amdgcn_exp2f((m1 - mm) * 1.4426950408889634f);
            const float wt = ((p & 1) ? e1 : e0) * __builtin_amdgcn_rcpf(e0 + e1);
            float v[4];
#pragma unroll
            for (int i = 0; i < 4; ++i) v[i] = xrow_sum(__uint_as_float(yv[i]) * wt);
            if (l < 16) {
#pragma unroll
                for (int i = 0; i < 4; ++i) s_r[(n * 4 + i) * 20 + w] = v[i];
            }
        }
        lds_barrier();
        float hval = 0.f;
        if (w == 0) {
            const f32x4 a = *(const f32x4*)&s_r[l * 20], bb = *(const f32x4*)&s_r[l * 20 + 4];
            const f32x4 c = *(const f32x4*)&s_r[l * 20 + 8], d = *(const f32x4*)&s_r[l * 20 + 12];
            const f32x4 e = (a + bb) + (c + d);
            hval = bfr(xres + bfr((e[0] + e[1]) + (e[2] + e[3])));
            const u32 pr = __builtin_amdgcn_perm(dppu<DPP_SHL1>(__float_as_uint(hval)), __float_as_uint(hval), 0x07060302u);
            const u32x4 hv = {canon(pr), canon(dppu<DPP_SHL2>(pr)), canon(dppu<DPP_SHL4>(pr)), canon(dppu<DPP_SHL6>(pr))};
            if ((l & 7) == 0) st16_sc1(hv, r_ws, OFF_H + wg * 128 + l * 2);
        }
        STAMP(9);
        if (w < 11) {
            const int pj = w * 4 + g;
            const int pjc = pj < NMLP ? pj : NMLP - 1;
            const u32 o = OFF_D + (u32)pjc * 8192u + (ch >> 1) * 2048 + (n >> 3) * 1024 + rb * 256 + (ch & 1) * 128 + (n & 7) * 16;
            u32x4 dv;
            for (u32 spin = 0; spin < SPIN_CAP; ++spin) {
                asm volatile("" ::: "memory");
                dv = ld16_sc1(r_ws, o);
                if (__all(full4(dv))) break;
            }
            STAMP(10);
            float v[4];
#pragma unroll
            for (int i = 0; i < 4; ++i) v[i] = xrow_sum(pj < NMLP ? __uint_as_float(dv[i]) : 0.f);
            if (l < 16) {
#pragma unroll
                for (int i = 0; i < 4; ++i) s_r2[((n & 7) * 8 + (n >> 3) * 4 + i) * 20 + w] = v[i];
            }
        }
        lds_barrier();
        if (w == 0) {
            const f32x4 a = *(const f32x4*)&s_r2[l * 20], bb = *(const f32x4*)&s_r2[l * 20 + 4], c = *(const f32x4*)&s_r2[l * 20 + 8];
            const f32x4 e = a + bb;
            const float sum = ((e[0] + e[1]) + (e[2] + e[3])) + ((c[0] + c[1]) + c[2]);
            const float o = hval + bfr(sum);
            out[rb * D + ch * 64 + l] = (u16)pk_bf16(o, o);
        }
        STAMP(11);
    } else if (is_mlp) {
        if (w < 4) {
            const int mat = w & 1, kh = w >> 1, kb = n >> 2, chunk = n & 3;
            const rsrc_t r = mk_rsrc(mat ? wu : wg_, D * F * 2);
            const u32 o = (u32)(kh * 256 + kb * 32 + g * 8) * (F * 2) + mj * 64 + chunk * 16;
            u32x4 w0[8], w1[8];
#pragma unroll
            for (int i = 0; i < 8; ++i) w0[i] = ld16(r, o, i * (F * 2), 1);
#pragma unroll
            for (int i = 0; i < 8; ++i) w1[i] = ld16(r, o, (128 + i) * (F * 2), 1);
            bf16x8 f0[8], f1[8];
#pragma unroll
            for (int c = 0; c < 8; ++c) { f0[c] = colfrag(w0, c); }
#pragma unroll
            for (int c = 0; c < 8; ++c) { f1[c] = colfrag(w1, c); }
            pin();
            lds_barrier();
            const int ab = n & 3, akb = n >> 2;
            const bf16x8 a0 = asfrag(*(const u32x4*)&s_hn[ab * 520 + kh * 256 + akb * 32 + g * 8]);
            const bf16x8 a1 = asfrag(*(const u32x4*)&s_hn[ab * 520 + kh * 256 + 128 + akb * 32 + g * 8]);
            f32x4 acc[8];
#pragma unroll
            for (int c = 0; c < 8; ++c) acc[c] = mfma(a0, f0[c], f32x4{0.f, 0.f, 0.f, 0.f});
#pragma unroll
            for (int c = 0; c < 8; ++c) acc[c] = mfma(a1, f1[c], acc[c]);
            if (kb == g) {
#pragma unroll
                for (int c = 0; c < 8; ++c) *(f32x4*)&s_gu[((mat * 8 + kh * 4 + kb) * 32 + chunk * 8 + c) * 4] = acc[c];
            }
            lds_barrier();
            if (w == 0) {
                f32x4 s = {0.f, 0.f, 0.f, 0.f};
                const int m2 = l >> 5, col = l & 31;
#pragma unroll
                for (int i = 0; i < 8; ++i) s += *(const f32x4*)&s_gu[((m2 * 8 + i) * 32 + col) * 4];
#pragma unroll
                for (int i = 0; i < 4; ++i) {
                    const float v = bfr(s[i]);
                    auto sw = __builtin_amdgcn_permlane32_swap(__float_as_uint(v), __float_as_uint(v), false, false);
                    const float pr = __uint_as_float(sw[0]) * __uint_as_float(sw[1]);
                    if (l < 32) s_a[i * 32 + col] = (u16)pk_bf16(pr, pr);
                }
            }
            lds_barrier();
        } else if (w < 8) {
            const int c4 = w - 4;
            const rsrc_t r = mk_rsrc(wd + (size_t)mj * 32 * D, 32 * D * 2);
            const u32 o = (u32)(g * 8) * 1024u + c4 * 256 + n * 16;
            u32x4 w0[8];
#pragma unroll
            for (int i = 0; i < 8; ++i) w0[i] = ld16(r, o + (i & 3) * 1024, (i >> 2) * 4096, 1);
            bf16x8 f0[8];
#pragma unroll
            for (int c = 0; c < 8; ++c) f0[c] = colfrag(w0, c);
            pin();
            lds_barrier();
            lds_barrier();
            lds_barrier();
            const bf16x8 aa = asfrag(*(const u32x4*)&s_a[(n >> 2) * 32 + g * 8]);
            float dp[8];
#pragma unroll
            for (int c = 0; c < 8; ++c) dp[c] = mfma(aa, f0[c], f32x4{0.f, 0.f, 0.f, 0.f})[0];
            const u32 so = OFF_D + (u32)mj * 8192u + c4 * 2048 + l * 16;
            st16_sc1(canon4(f32x4{dp[0], dp[1], dp[2], dp[3]}), r_ws, so);
            st16_sc1(canon4(f32x4{dp[4], dp[5], dp[6], dp[7]}), r_ws, so + 1024);
            STAMP(11);
        } else if (w >= 12) {
            const int hb = w - 12;
            u32x4 hv;
            for (u32 spin = 0; spin < SPIN_CAP; ++spin) {
                asm volatile("" ::: "memory");
                hv = ld16_sc1(r_ws, OFF_H + hb * 1024 + l * 16);
                if (__all(full4(hv))) break;
            }
            STAMP(8);
            float mb, rs;
            ln_stats(hv, mb, rs);
            *(u32x4*)&s_hn[hb * 520 + l * 8] = ln_apply(hv, mb, rs);
            STAMP(9);
            lds_barrier();
            lds_barrier();
            lds_barrier();
        } else {
            lds_barrier();
            lds_barrier();
            lds_barrier();
        }
    }
    STAMP(12);
    STAMP_FLUSH;
}

extern "C" void kernel_launch(void* const* d_in, const int* in_sizes, int n_in, void* d_out, int out_size, void* d_ws,
                              size_t ws_size, hipStream_t stream) {
    fused_block<<<NWG, 1024, 0, stream>>>((const u16*)d_in[0], (const u16*)d_in[1], (const u16*)d_in[2], (const u16*)d_in[3],
                                          (const u16*)d_in[4], (const u16*)d_in[6], (const u16*)d_in[7], (const u16*)d_in[8],
                                          (u16*)d_out, (char*)d_ws);
}
```

```cpp
#include <hip/hip_runtime.h>
#include <stdint.h>

typedef unsigned short u16;
typedef unsigned int u32;
typedef unsigned long long u64;
typedef __attribute__((ext_vector_type(2))) u32 u32x2;
typedef __attribute__((ext_vector_type(4))) u32 u32x4;
typedef __attribute__((ext_vector_type(2))) float f32x2;
typedef __attribute__((ext_vector_type(4))) float f32x4;
typedef __attribute__((ext_vector_type(2))) __bf16 bf16x2;
typedef __attribute__((ext_vector_type(8))) __bf16 bf16x8;
typedef __amdgpu_buffer_rsrc_t rsrc_t;

constexpr int NB = 4, D = 512, H = 32, DH = 128, SK = 4096, W = 1024, F = 1376;
constexpr int NWG = 256, NRED = 32, NMLP = 43;

constexpr u32 OFF_E = 0;
constexpr u32 OFF_FLAG = 1024;
constexpr u32 OFF_H = 4096;
constexpr u32 OFF_O = 8192;
constexpr u32 O_STRIDE = 640;
constexpr u32 OFF_Q = OFF_O + NWG * O_STRIDE;
constexpr u32 OFF_Y = OFF_Q + NWG * 2048;
constexpr u32 OFF_D = OFF_Y + NWG * 2048;
constexpr u32 WS_USED = OFF_D + NMLP * 8192;
constexpr u32 OFF_TS = 2u << 20;

constexpr u32 SENT = 0xFFFFFFFFu;
constexpr int AUX_NT = 2, AUX_SC1 = 16;
constexpr u32 SPIN_CAP = 1u << 18;

__device__ __forceinline__ rsrc_t mk_rsrc(const void* p, u32 bytes) {
    return __builtin_amdgcn_make_buffer_rsrc((void*)p, 0, (int)bytes, 0x00020000);
}
#define WQ_AUX 2
#define WO_AUX 2
template <int AUX> __device__ __forceinline__ u32x4 ldx(rsrc_t r, u32 voff, u32 soff) { return __builtin_amdgcn_raw_buffer_load_b128(r, voff, soff, AUX); }
__device__ __forceinline__ u32x4 ld16(rsrc_t r, u32 voff, u32 soff, const int aux_nt) {
    return aux_nt ? __builtin_amdgcn_raw_buffer_load_b128(r, voff, soff, AUX_NT)
                  : __builtin_amdgcn_raw_buffer_load_b128(r, voff, soff, 0);
}
__device__ __forceinline__ u32x4 ld16_sc1(rsrc_t r, u32 voff) { return __builtin_amdgcn_raw_buffer_load_b128(r, voff, 0, AUX_SC1); }
__device__ __forceinline__ u32x2 ld8_sc1(rsrc_t r, u32 voff) { return __builtin_amdgcn_raw_buffer_load_b64(r, voff, 0, AUX_SC1); }
__device__ __forceinline__ void st16_sc1(u32x4 v, rsrc_t r, u32 voff) { __builtin_amdgcn_raw_buffer_store_b128(v, r, voff, 0, AUX_SC1); }
__device__ __forceinline__ void st8_sc1(u32x2 v, rsrc_t r, u32 voff) { __builtin_amdgcn_raw_buffer_store_b64(v, r, voff, 0, AUX_SC1); }

__device__ __forceinline__ float bf_lo(u32 v) { return __uint_as_float(v << 16); }
__device__ __forceinline__ float bf_hi(u32 v) { return __uint_as_float(v & 0xffff0000u); }
__device__ __forceinline__ u32 pk_bf16(float a, float b) {
    f32x2 v = {a, b};
    return __builtin_bit_cast(u32, __builtin_convertvector(v, bf16x2));
}
__device__ __forceinline__ float bfr(float a) { return bf_lo(pk_bf16(a, a)); }
__device__ __forceinline__ u32 canon(u32 v) { return v < SENT ? v : SENT - 1; }
__device__ __forceinline__ u32x4 canon4(f32x4 v) {
    u32x4 r;
#pragma unroll
    for (int i = 0; i < 4; ++i) r[i] = canon(__float_as_uint(v[i]));
    return r;
}
__device__ __forceinline__ u32 umax(u32 a, u32 b) { return a > b ? a : b; }
__device__ __forceinline__ u32 umax4(u32x4 v) { return umax(umax(v[0], v[1]), umax(v[2], v[3])); }
__device__ __forceinline__ bool full4(u32x4 v) { return umax4(v) != SENT; }
typedef __attribute__((address_space(3))) const u32 lds_cu32;
__device__ __forceinline__ u32x4 lds_poll16(const u32* p) {
    u32x4 v;
    asm volatile("ds_read_b128 %0, %1\n\ts_waitcnt lgkmcnt(0)" : "=v"(v) : "v"((u32)(size_t)(lds_cu32*)p) : "memory");
    return v;
}
__device__ __forceinline__ void keep(bf16x8& v) {
    u32x4 r = __builtin_bit_cast(u32x4, v);
    asm volatile("" : "+v"(r));
    v = __builtin_bit_cast(bf16x8, r);
}

template <int CTRL> __device__ __forceinline__ float dpp(float v) {
    return __uint_as_float(__builtin_amdgcn_update_dpp(0u, __float_as_uint(v), CTRL, 0xf, 0xf, true));
}
template <int CTRL> __device__ __forceinline__ u32 dppu(u32 v) { return __builtin_amdgcn_update_dpp(0u, v, CTRL, 0xf, 0xf, true); }
constexpr int DPP_X1 = 0xB1, DPP_X2 = 0x4E, DPP_HMIR = 0x141, DPP_MIR = 0x140;
constexpr int DPP_ROR8 = 0x128;
constexpr int DPP_SHL1 = 0x101, DPP_SHL2 = 0x102, DPP_SHL4 = 0x104, DPP_SHL6 = 0x106;

__device__ __forceinline__ float row8_sum(float v) { v += dpp<DPP_X1>(v); v += dpp<DPP_X2>(v); v += dpp<DPP_HMIR>(v); return v; }
__device__ __forceinline__ float row16_sum(float v) { v = row8_sum(v); v += dpp<DPP_MIR>(v); return v; }
__device__ __forceinline__ float row16_max(float v) {
    v = fmaxf(v, dpp<DPP_X1>(v)); v = fmaxf(v, dpp<DPP_X2>(v));
    v = fmaxf(v, dpp<DPP_HMIR>(v)); v = fmaxf(v, dpp<DPP_MIR>(v));
    return v;
}
__device__ __forceinline__ float xrow_sum(float x) {
    auto s = __builtin_amdgcn_permlane16_swap(__float_as_uint(x), __float_as_uint(x), false, false);
    x = __uint_as_float(s[0]) + __uint_as_float(s[1]);
    auto t = __builtin_amdgcn_permlane32_swap(__float_as_uint(x), __float_as_uint(x), false, false);
    return __uint_as_float(t[0]) + __uint_as_float(t[1]);
}
__device__ __forceinline__ float xrow_max(float x) {
    auto s = __builtin_amdgcn_permlane16_swap(__float_as_uint(x), __float_as_uint(x), false, false);
    x = fmaxf(__uint_as_float(s[0]), __uint_as_float(s[1]));
    auto t = __builtin_amdgcn_permlane32_swap(__float_as_uint(x), __float_as_uint(x), false, false);
    return fmaxf(__uint_as_float(t[0]), __uint_as_float(t[1]));
}
__device__ __forceinline__ float wave_sum(float v) { return xrow_sum(row16_sum(v)); }

__device__ __forceinline__ void lds_barrier() { asm volatile("s_waitcnt lgkmcnt(0)\n\ts_barrier" ::: "memory"); }
__device__ __forceinline__ void pin() { __builtin_amdgcn_sched_barrier(0); }

__device__ __forceinline__ void unpack8(u32x4 v, float* f) {
#pragma unroll
    for (int j = 0; j < 4; ++j) { f[2 * j] = bf_lo(v[j]); f[2 * j + 1] = bf_hi(v[j]); }
}

__device__ __forceinline__ void ln_stats(u32x4 row, float& mb, float& rs) {
    float f[8];
    unpack8(row, f);
    float s = ((f[0] + f[1]) + (f[2] + f[3])) + ((f[4] + f[5]) + (f[6] + f[7]));
    float mean = wave_sum(s) * (1.f / D);
    float q = 0.f;
#pragma unroll
    for (int i = 0; i < 8; ++i) { float d = f[i] - mean; q += d * d; }
    float var = wave_sum(q) * (1.f / D);
    mb = bfr(mean);
    float sq = bfr(__builtin_amdgcn_sqrtf(bfr(bfr(var) + 1e-5f)));
    rs = __builtin_amdgcn_rcpf(sq);
}
__device__ __forceinline__ u32x4 ln_apply(u32x4 v, float mb, float rs) {
    u32x4 r;
#pragma unroll
    for (int j = 0; j < 4; ++j) {
        u32 d = pk_bf16(bf_lo(v[j]) - mb, bf_hi(v[j]) - mb);
        r[j] = pk_bf16(bf_lo(d) * rs, bf_hi(d) * rs);
    }
    return r;
}

__device__ __forceinline__ bf16x8 colfrag(const u32x4* in, int c) {
    const u32 sel = (c & 1) ? 0x07060302u : 0x05040100u;
    u32x4 r;
#pragma unroll
    for (int q = 0; q < 4; ++q) r[q] = __builtin_amdgcn_perm(in[2 * q + 1][c >> 1], in[2 * q][c >> 1], sel);
    return __builtin_bit_cast(bf16x8, r);
}
__device__ __forceinline__ float dot2(u32 a, u32 b, float c) {
    return __builtin_amdgcn_fdot2_f32_bf16(__builtin_bit_cast(bf16x2, a), __builtin_bit_cast(bf16x2, b), c, false);
}
__device__ __forceinline__ f32x4 mfma(bf16x8 a, bf16x8 b, f32x4 c) { return __builtin_amdgcn_mfma_f32_16x16x32_bf16(a, b, c, 0, 0, 0); }
__device__ __forceinline__ bf16x8 asfrag(u32x4 v) { return __builtin_bit_cast(bf16x8, v); }

#define STAMP(k) do { } while (0)
#define STAMP_INIT do { } while (0)
#define STAMP_FLUSH do { } while (0)

#define PREF 1
#define KPRE 2

__global__ __launch_bounds__(1024) void fused_block(const u16* __restrict__ x, const u16* __restrict__ kc, const u16* __restrict__ vc,
                                                    const u16* __restrict__ wq, const u16* __restrict__ wo, const u16* __restrict__ wg_,
                                                    const u16* __restrict__ wu, const u16* __restrict__ wd, u16* __restrict__ out,
                                                    char* __restrict__ ws) {
    __shared__ __attribute__((aligned(16))) u32 s_qb[64];
    __shared__ __attribute__((aligned(16))) float s_o[16 * 132];
    __shared__ __attribute__((aligned(16))) f32x2 s_ml[16];
    __shared__ __attribute__((aligned(16))) float s_r[64 * 20];
    __shared__ __attribute__((aligned(16))) float s_r2[64 * 20];
    __shared__ __attribute__((aligned(16))) u16 s_hn[4 * 520];
    __shared__ __attribute__((aligned(16))) float s_gu[2 * 16 * 32 * 4];
    __shared__ __attribute__((aligned(16))) u16 s_a[4 * 32];

    const int t = threadIdx.x, l = t & 63, g = l >> 4, n = l & 15;
    const int w = __builtin_amdgcn_readfirstlane(t >> 6);
    const int wg = blockIdx.x;
    const int h = (wg & 7) * 4 + (wg >> 6), jj = (wg >> 3) & 7, b = jj >> 1, half = jj & 1;
    const int bh = b * H + h, gi = h * 8 + jj;
    const bool is_red = wg < NRED, is_mlp = wg >= NRED && wg < NRED + NMLP;
    const int mx7 = wg & 7;
    const int mj = mx7 * 5 + (mx7 < 3 ? mx7 : 3) + ((wg - NRED) >> 3);

    const rsrc_t r_ws = mk_rsrc(ws, WS_USED);
    STAMP_INIT;
    STAMP(0);

    const size_t kvbase = ((size_t)bh * SK + (SK - W) + half * 512) * DH;
    const u32 kvo = w * 8192 + l * 16;
    const rsrc_t r_k = mk_rsrc(kc + kvbase, 512 * DH * 2);
    const rsrc_t r_v = mk_rsrc(vc + kvbase, 512 * DH * 2);
    u32x4 kv[8], vv[8];
    u32 tg0 = 0, tg1 = 0;

    if (w == 0) {
        __builtin_amdgcn_s_setprio(3);
        s_qb[l] = SENT;
        const rsrc_t r_x = mk_rsrc(x, NB * D * 2);
        u32x4 xs[4], xf[2], wv[16];
#pragma unroll
        for (int i = 0; i < 4; ++i) xs[i] = ld16(r_x, (n >> 2) * 1024 + ((n & 3) * 4 + g) * 64 + i * 16, 0, 0);
#pragma unroll
        for (int s = 0; s < 2; ++s) xf[s] = ld16(r_x, (n >> 2) * 1024 + jj * 128 + s * 64 + g * 16, 0, 0);
        const u32x2 ev = ld8_sc1(r_ws, OFF_E);
        {
            const rsrc_t r = mk_rsrc(wq + h * DH, (D * H * DH - h * DH) * 2);
            const u32 o = (u32)(jj * 64 + g * 8) * 8192u + n * 16;
#pragma unroll
            for (int i = 0; i < 16; ++i) wv[i] = ldx<WQ_AUX>(r, o, ((i >> 3) * 32 + (i & 7)) * 8192);
        }
        pin();
        lds_barrier();
        pin();
        float f[32];
#pragma unroll
        for (int i = 0; i < 4; ++i) unpack8(xs[i], f + 8 * i);
        float s0 = 0.f, s1 = 0.f, s2 = 0.f, s3 = 0.f;
#pragma unroll
        for (int i = 0; i < 8; ++i) { s0 += f[i]; s1 += f[8 + i]; s2 += f[16 + i]; s3 += f[24 + i]; }
        float sm = (s0 + s1) + (s2 + s3);
        sm += dpp<DPP_X1>(sm); sm += dpp<DPP_X2>(sm);
        const float mean = xrow_sum(sm) * (1.f / D);
        s0 = s1 = s2 = s3 = 0.f;
#pragma unroll
        for (int i = 0; i < 8; ++i) {
            const float d0 = f[i] - mean, d1 = f[8 + i] - mean, d2 = f[16 + i] - mean, d3 = f[24 + i] - mean;
            s0 += d0 * d0; s1 += d1 * d1; s2 += d2 * d2; s3 += d3 * d3;
        }
        float sq = (s0 + s1) + (s2 + s3);
        sq += dpp<DPP_X1>(sq); sq += dpp<DPP_X2>(sq);
        const float var = xrow_sum(sq) * (1.f / D);
        const float mb = bfr(mean);
        const float rs = __builtin_amdgcn_rcpf(bfr(__builtin_amdgcn_sqrtf(bfr(bfr(var) + 1e-5f))));
        const bf16x8 xa0 = asfrag(ln_apply(xf[0], mb, rs)), xa1 = asfrag(ln_apply(xf[1], mb, rs));
        STAMP(1);
        float qp[8];
#pragma unroll
        for (int c = 0; c < 8; ++c) {
            f32x4 a = mfma(xa0, colfrag(wv, c), f32x4{0.f, 0.f, 0.f, 0.f});
            qp[c] = mfma(xa1, colfrag(wv + 8, c), a)[0];
        }
        const u32 qo = OFF_Q + gi * 2048 + l * 16;
        st16_sc1(u32x4{__float_as_uint(qp[0]), __float_as_uint(qp[1]), __float_as_uint(qp[2]), __float_as_uint(qp[3])}, r_ws, qo);
        st16_sc1(u32x4{__float_as_uint(qp[4]), __float_as_uint(qp[5]), __float_as_uint(qp[6]), __float_as_uint(qp[7])}, r_ws, qo + 1024);
        {
            const u32x4 s4 = {SENT, SENT, SENT, SENT};
            st16_sc1(s4, r_ws, OFF_Y + gi * 2048 + l * 16);
            st16_sc1(s4, r_ws, OFF_Y + gi * 2048 + 1024 + l * 16);
            if (l < 33) st16_sc1(s4, r_ws, OFF_O + gi * O_STRIDE + l * 16);
            if (is_red && l < 8) st16_sc1(s4, r_ws, OFF_H + wg * 128 + l * 16);
            if (is_mlp) {
#pragma unroll
                for (int i = 0; i < 8; ++i) st16_sc1(s4, r_ws, OFF_D + mj * 8192 + i * 1024 + l * 16);
            }
        }
        {
            const u64 e = ((u64)(u32)__builtin_amdgcn_readfirstlane(ev[1]) << 32) | (u32)__builtin_amdgcn_readfirstlane(ev[0]);
            const u64 tag = e * 6364136223846793005ull + 1442695040888963407ull;
            tg0 = (u32)tag; tg1 = (u32)(tag >> 32);
        }
        asm volatile("s_waitcnt vmcnt(0)" ::: "memory");
        if (l == 0) st8_sc1(u32x2{tg0, tg1}, r_ws, OFF_FLAG + gi * 8);
        STAMP(2);
        for (u32 spin = 0; spin < SPIN_CAP; ++spin) {
            asm volatile("" ::: "memory");
            const u32x4 fl = ld16_sc1(r_ws, OFF_FLAG + h * 64 + (l & 3) * 16);
            if (__all(((fl[0] ^ tg0) | (fl[1] ^ tg1) | (fl[2] ^ tg0) | (fl[3] ^ tg1)) == 0)) break;
        }
        asm volatile("" ::: "memory");
        STAMP(3);
        {
            const u32 o = OFF_Q + (u32)(h * 8 + (g >> 1)) * 2048u + (g & 1) * 1024 + (b * 16 + n) * 16;
            u32x4 qv[4];
#pragma unroll
            for (int i = 0; i < 4; ++i) qv[i] = ld16_sc1(r_ws, o + i * 4096);
            float q4[4];
#pragma unroll
            for (int j = 0; j < 4; ++j) {
                const float v = (__uint_as_float(qv[0][j]) + __uint_as_float(qv[1][j])) + (__uint_as_float(qv[2][j]) + __uint_as_float(qv[3][j]));
                auto sw = __builtin_amdgcn_permlane32_swap(__float_as_uint(v), __float_as_uint(v), false, false);
                q4[j] = __uint_as_float(sw[0]) + __uint_as_float(sw[1]);
            }
            if (l < 32) *(u32x2*)&s_qb[n * 4 + g * 2] = u32x2{canon(pk_bf16(q4[0], q4[1])), canon(pk_bf16(q4[2], q4[3]))};
        }
        pin();
#pragma unroll
        for (int i = 0; i < KPRE; ++i) kv[i] = ld16(r_k, kvo + (i & 3) * 1024, (i >> 2) * 4096, 1);
    } else {
#pragma unroll
        for (int i = 0; i < KPRE; ++i) kv[i] = ld16(r_k, kvo + (i & 3) * 1024, (i >> 2) * 4096, 1);
        pin();
        asm volatile("s_barrier" ::: "memory");
    }
    pin();
#pragma unroll
    for (int i = KPRE; i < 8; ++i) kv[i] = ld16(r_k, kvo + (i & 3) * 1024, (i >> 2) * 4096, 1);
#pragma unroll
    for (int i = 0; i < 8; ++i) vv[i] = ld16(r_v, kvo + (i & 3) * 1024, (i >> 2) * 4096, 1);
    u32x4 fl0 = {0, 0, 0, 0}, fl1 = {0, 0, 0, 0};
    const bool chk = (w == 0) && (is_red || is_mlp);
    if (chk) { fl0 = ld16_sc1(r_ws, OFF_FLAG + l * 16); fl1 = ld16_sc1(r_ws, OFF_FLAG + 1024 + l * 16); }
    pin();
    STAMP(4);

    u32x4 qf;
    for (u32 spin = 0; spin < SPIN_CAP; ++spin) {
        qf = lds_poll16(&s_qb[n * 4]);
        if (__all(full4(qf))) break;
        __builtin_amdgcn_s_sleep(2);
    }
    STAMP(5);

    float sc[8];
#pragma unroll
    for (int i = 0; i < 8; ++i) {
        float a = 0.f;
#pragma unroll
        for (int j = 0; j < 4; ++j)
            a = dot2(kv[i][j], qf[j], a);
        sc[i] = row16_sum(a);
    }
#pragma unroll
    for (int i = 0; i < 8; i += 2) {
        u32 p = pk_bf16(sc[i], sc[i + 1]);
        p = pk_bf16(bf_lo(p) * 0.08838834764831845f, bf_hi(p) * 0.08838834764831845f);
        sc[i] = bf_lo(p); sc[i + 1] = bf_hi(p);
    }
    float mx = fmaxf(fmaxf(fmaxf(sc[0], sc[1]), fmaxf(sc[2], sc[3])), fmaxf(fmaxf(sc[4], sc[5]), fmaxf(sc[6], sc[7])));
    mx = xrow_max(mx);
    u32x4 pa;
    float ls = 0.f;
#pragma unroll
    for (int i = 0; i < 8; i += 2) {
        const float p0 = __builtin_amdgcn_exp2f((sc[i] - mx) * 1.4426950408889634f);
        const float p1 = __builtin_amdgcn_exp2f((sc[i + 1] - mx) * 1.4426950408889634f);
        ls += p0 + p1;
        pa[i >> 1] = pk_bf16(p0, p1);
    }
    ls = xrow_sum(ls);
    if (l == 0) s_ml[w] = f32x2{mx, ls};
    pin();
    STAMP(6);

    {
        float op[8];
#pragma unroll
        for (int c = 0; c < 8; ++c) op[c] = mfma(asfrag(pa), colfrag(vv, c), f32x4{0.f, 0.f, 0.f, 0.f})[0];
        if (l < 16) {
            *(f32x4*)&s_o[w * 132 + n * 8] = f32x4{op[0], op[1], op[2], op[3]};
            *(f32x4*)&s_o[w * 132 + n * 8 + 4] = f32x4{op[4], op[5], op[6], op[7]};
        }
    }
    pin();
    STAMP(7);
    const int kh = jj >> 2, cq = jj & 3;
    u32x4 wb[16];
    if (w == 1) {
        const rsrc_t r = mk_rsrc(wo + (size_t)(h * DH + kh * 64) * D + cq * 128, (64 * D - cq * 128) * 2);
        const u32 o = (u32)g * 8192u + n * 16;
#pragma unroll
        for (int i = 0; i < 16; ++i) wb[i] = ldx<WO_AUX>(r, o + (i & 3) * 1024, (i >> 2) * 4096 + (i >> 3) * 24576);
    }
    if (chk) {
        for (u32 spin = 0; spin < SPIN_CAP; ++spin) {
            const bool ok = ((fl0[0] ^ tg0) | (fl0[1] ^ tg1) | (fl0[2] ^ tg0) | (fl0[3] ^ tg1) |
                             (fl1[0] ^ tg0) | (fl1[1] ^ tg1) | (fl1[2] ^ tg0) | (fl1[3] ^ tg1)) == 0;
            if (__all(ok)) break;
            asm volatile("" ::: "memory");
            fl0 = ld16_sc1(r_ws, OFF_FLAG + l * 16); fl1 = ld16_sc1(r_ws, OFF_FLAG + 1024 + l * 16);
        }
        if (wg == 0 && l == 0) st8_sc1(u32x2{tg0, tg1}, r_ws, OFF_E);
    }
    pin();
    lds_barrier();
    STAMP(8);

    u32 pv = 0;
    if (w >= 4 && w < 8) {
        const int u = w - 4;
        const f32x2 ml = s_ml[n];
        const f32x4 a = *(const f32x4*)&s_o[n * 132 + u * 32 + g * 8], a2 = *(const f32x4*)&s_o[n * 132 + u * 32 + g * 8 + 4];
        const float M = row16_max(ml[0]);
        const float f = __builtin_amdgcn_exp2f((ml[0] - M) * 1.4426950408889634f);
        f32x4 v, v2;
#pragma unroll
        for (int i = 0; i < 4; ++i) { v[i] = row16_sum(a[i] * f); v2[i] = row16_sum(a2[i] * f); }
        const float L = row16_sum(ml[1] * f);
        if (n < 2) st16_sc1(canon4(n ? v2 : v), r_ws, OFF_O + gi * O_STRIDE + (u * 32 + g * 8 + n * 4) * 4);
        if (u == 0 && l == 0) st8_sc1(u32x2{canon(__float_as_uint(M)), canon(__float_as_uint(L))}, r_ws, OFF_O + gi * O_STRIDE + 512);
        STAMP(9);
    } else if (w == 1) {
        __builtin_amdgcn_s_setprio(3);
        bf16x8 b0[8], b1[8];
#pragma unroll
        for (int c = 0; c < 8; ++c) { b0[c] = colfrag(wb, c); b1[c] = colfrag(wb + 8, c); keep(b0[c]); keep(b1[c]); }
        pin();
        const u32 so = OFF_O + (u32)(h * 8 + (n >> 2) * 2 + (n & 1)) * O_STRIDE;
        const u32 oo = so + (kh * 64 + ((n >> 1) & 1) * 32 + g * 8) * 4;
        u32x4 o0, o1;
        u32x2 ml;
        for (u32 spin = 0; spin < SPIN_CAP; ++spin) {
            asm volatile("" ::: "memory");
            o0 = ld16_sc1(r_ws, oo); o1 = ld16_sc1(r_ws, oo + 16); ml = ld8_sc1(r_ws, so + 512);
            if (__all(umax(umax(umax4(o0), umax4(o1)), umax(ml[0], ml[1])) != SENT)) break;
        }
        STAMP(9);
        const float m = __uint_as_float(ml[0]);
        const float M = fmaxf(m, dpp<DPP_X1>(m));
        const float e = __builtin_amdgcn_exp2f((m - M) * 1.4426950408889634f);
        const float le = __uint_as_float(ml[1]) * e;
        const float wt = e * __builtin_amdgcn_rcpf(le + dpp<DPP_X1>(le));
        float v[8];
#pragma unroll
        for (int i = 0; i < 4; ++i) { v[i] = __uint_as_float(o0[i]) * wt; v[4 + i] = __uint_as_float(o1[i]) * wt; }
#pragma unroll
        for (int i = 0; i < 8; ++i) v[i] += dpp<DPP_X1>(v[i]);
        const bf16x8 oa = asfrag(u32x4{pk_bf16(v[0], v[1]), pk_bf16(v[2], v[3]), pk_bf16(v[4], v[5]), pk_bf16(v[6], v[7])});
        float yp[8];
#pragma unroll
        for (int c = 0; c < 8; ++c)
            yp[c] = mfma(oa, b0[c], f32x4{0.f, 0.f, 0.f, 0.f})[0] + mfma(oa, b1[c], f32x4{0.f, 0.f, 0.f, 0.f})[2];
        const u32 yo = OFF_Y + gi * 2048 + l * 16;
        st16_sc1(canon4(f32x4{yp[0], yp[1], yp[2], yp[3]}), r_ws, yo);
        st16_sc1(canon4(f32x4{yp[4], yp[5], yp[6], yp[7]}), r_ws, yo + 1024);
        STAMP(10);
    } else if (PREF && w >= 8 && w < 14) {
        const int u = wg >> 3, m0 = mx7 * 5 + (mx7 < 3 ? mx7 : 3), nm = mx7 < 3 ? 6 : 5;
        if (w < 12) {
            const int seg = l & 7, row = u * 16 + ((w - 8) >> 1) * 8 + (l >> 3);
            const rsrc_t r = mk_rsrc((w & 1) ? wu : wg_, D * F * 2);
            if (seg < nm) pv = __builtin_amdgcn_raw_buffer_load_b32(r, (u32)row * (F * 2) + (m0 + seg) * 64, 0, 0);
        } else {
            const int row = u * nm + (w - 12) * 4 + g;
            const rsrc_t r = mk_rsrc(wd, F * D * 2);
            if ((w - 12) * 4 + g < nm) pv = __builtin_amdgcn_raw_buffer_load_b32(r, (u32)(m0 * 32 + row) * 1024u + n * 64, 0, 0);
        }
    }
    if (is_red || is_mlp) lds_barrier();
    pin();

    if (is_red) {
        const int rb = wg >> 3, ch = wg & 7;
        float xres = 0.f;
        if (w == 0) xres = __uint_as_float((u32)x[rb * D + ch * 64 + l] << 16);
        const int p = w * 4 + g;
        const u32 sub = (n >> 3) * 1024 + rb * 256 + (ch & 1) * 128 + (n & 7) * 16;
        const u32 yo = OFF_Y + (u32)((p >> 1) * 8 + (p & 1) * 4 + (ch >> 1)) * 2048u + sub;
        u32x4 yv;
        for (u32 spin = 0; spin < SPIN_CAP; ++spin) {
            asm volatile("" ::: "memory");
            yv = ld16_sc1(r_ws, yo);
            if (__all(full4(yv))) break;
        }
        STAMP(11);
        {
            float v[4];
#pragma unroll
            for (int i = 0; i < 4; ++i) v[i] = xrow_sum(__uint_as_float(yv[i]));
            if (l < 16) {
#pragma unroll
                for (int i = 0; i < 4; ++i) s_r[((n & 7) * 8 + (n >> 3) * 4 + i) * 20 + w] = v[i];
            }
        }
        lds_barrier();
        float hval = 0.f;
        if (w == 0) {
            const f32x4 a = *(const f32x4*)&s_r[l * 20], bb = *(const f32x4*)&s_r[l * 20 + 4];
            const f32x4 c = *(const f32x4*)&s_r[l * 20 + 8], d = *(const f32x4*)&s_r[l * 20 + 12];
            const f32x4 e = (a + bb) + (c + d);
            hval = bfr(xres + bfr((e[0] + e[1]) + (e[2] + e[3])));
            const u32 pr = __builtin_amdgcn_perm(dppu<DPP_SHL1>(__float_as_uint(hval)), __float_as_uint(hval), 0x07060302u);
            const u32x4 hv = {canon(pr), canon(dppu<DPP_SHL2>(pr)), canon(dppu<DPP_SHL4>(pr)), canon(dppu<DPP_SHL6>(pr))};
            if ((l & 7) == 0) st16_sc1(hv, r_ws, OFF_H + wg * 128 + l * 2);
        }
        STAMP(12);
        if (w < 11) {
            const int pj = w * 4 + g;
            const int pjc = pj < NMLP ? pj : NMLP - 1;
            const u32 o = OFF_D + (u32)pjc * 8192u + (ch >> 1) * 2048 + sub;
            u32x4 dv;
            for (u32 spin = 0; spin < SPIN_CAP; ++spin) {
                asm volatile("" ::: "memory");
                dv = ld16_sc1(r_ws, o);
                if (__all(full4(dv))) break;
            }
            STAMP(13);
            float v[4];
#pragma unroll
            for (int i = 0; i < 4; ++i) v[i] = xrow_sum(pj < NMLP ? __uint_as_float(dv[i]) : 0.f);
            if (l < 16) {
#pragma unroll
                for (int i = 0; i < 4; ++i) s_r2[((n & 7) * 8 + (n >> 3) * 4 + i) * 20 + w] = v[i];
            }
        }
        lds_barrier();
        if (w == 0) {
            const f32x4 a = *(const f32x4*)&s_r2[l * 20], bb = *(const f32x4*)&s_r2[l * 20 + 4], c = *(const f32x4*)&s_r2[l * 20 + 8];
            const f32x4 e = a + bb;
            const float sum = ((e[0] + e[1]) + (e[2] + e[3])) + ((c[0] + c[1]) + c[2]);
            const float o = hval + bfr(sum);
            out[rb * D + ch * 64 + l] = (u16)pk_bf16(o, o);
        }
        STAMP(14);
    } else if (is_mlp) {
        if (w >= 8) {
            const int mat = w & 1, kq = (w - 8) >> 1, kb = n >> 2, chunk = n & 3;
            const rsrc_t r = mk_rsrc(mat ? wu : wg_, D * F * 2);
            const u32 o = (u32)(kq * 128 + kb * 32 + g * 8) * (F * 2) + mj * 64 + chunk * 16;
            u32x4 w0[8];
#pragma unroll
            for (int i = 0; i < 8; ++i) w0[i] = ld16(r, o, i * (F * 2), 0);
            bf16x8 f0[8];
#pragma unroll
            for (int c = 0; c < 8; ++c) { f0[c] = colfrag(w0, c); keep(f0[c]); }
            pin();
            STAMP(11);
            lds_barrier();
            const int ab = n & 3, akb = n >> 2;
            const bf16x8 a0 = asfrag(*(const u32x4*)&s_hn[ab * 520 + kq * 128 + akb * 32 + g * 8]);
            f32x4 acc[8];
#pragma unroll
            for (int c = 0; c < 8; ++c) acc[c] = mfma(a0, f0[c], f32x4{0.f, 0.f, 0.f, 0.f});
            if (kb == g) {
#pragma unroll
                for (int c = 0; c < 8; ++c) *(f32x4*)&s_gu[((mat * 16 + kq * 4 + kb) * 32 + chunk * 8 + c) * 4] = acc[c];
            }
            lds_barrier();
            lds_barrier();
        } else if (w >= 4) {
            const int c4 = w - 4;
            const rsrc_t r = mk_rsrc(wd + (size_t)mj * 32 * D, 32 * D * 2);
            const u32 o = (u32)(g * 8) * 1024u + c4 * 256 + n * 16;
            u32x4 w0[8];
#pragma unroll
            for (int i = 0; i < 8; ++i) w0[i] = ld16(r, o + (i & 3) * 1024, (i >> 2) * 4096, 0);
            bf16x8 f0[8];
#pragma unroll
            for (int c = 0; c < 8; ++c) { f0[c] = colfrag(w0, c); keep(f0[c]); }
            pin();
            STAMP(11);
            lds_barrier();
            lds_barrier();
            lds_barrier();
            const bf16x8 aa = asfrag(*(const u32x4*)&s_a[(n >> 2) * 32 + g * 8]);
            float dp[8];
#pragma unroll
            for (int c = 0; c < 8; ++c) dp[c] = mfma(aa, f0[c], f32x4{0.f, 0.f, 0.f, 0.f})[0];
            const u32 so = OFF_D + (u32)mj * 8192u + c4 * 2048 + l * 16;
            st16_sc1(canon4(f32x4{dp[0], dp[1], dp[2], dp[3]}), r_ws, so);
            st16_sc1(canon4(f32x4{dp[4], dp[5], dp[6], dp[7]}), r_ws, so + 1024);
            STAMP(14);
        } else {
            const int hb = w;
            u32x4 hv;
            for (u32 spin = 0; spin < SPIN_CAP; ++spin) {
                asm volatile("" ::: "memory");
                hv = ld16_sc1(r_ws, OFF_H + hb * 1024 + l * 16);
                if (__all(full4(hv))) break;
            }
            STAMP(11);
            float mb, rs;
            ln_stats(hv, mb, rs);
            *(u32x4*)&s_hn[hb * 520 + l * 8] = ln_apply(hv, mb, rs);
            STAMP(12);
            lds_barrier();
            lds_barrier();
            {
                const int col = w * 8 + (n & 7), mat = n >> 3;
                f32x4 s = *(const f32x4*)&s_gu[((mat * 16 + g * 4) * 32 + col) * 4];
#pragma unroll
                for (int i = 1; i < 4; ++i) s += *(const f32x4*)&s_gu[((mat * 16 + g * 4 + i) * 32 + col) * 4];
#pragma unroll
                for (int i = 0; i < 4; ++i) {
                    const float v = bfr(xrow_sum(s[i]));
                    const float pr = v * dpp<DPP_ROR8>(v);
                    if (l < 8) s_a[i * 32 + col] = (u16)pk_bf16(pr, pr);
                }
            }
            STAMP(13);
            lds_barrier();
        }
    }
    asm volatile("" :: "v"(pv));
    STAMP(15);
    STAMP_FLUSH;
}

extern "C" void kernel_launch(void* const* d_in, const int* in_sizes, int n_in, void* d_out, int out_size, void* d_ws,
                              size_t ws_size, hipStream_t stream) {
    fused_block<<<NWG, 1024, 0, stream>>>((const u16*)d_in[0], (const u16*)d_in[1], (const u16*)d_in[2], (const u16*)d_in[3],
                                          (const u16*)d_in[4], (const u16*)d_in[6], (const u16*)d_in[7], (const u16*)d_in[8],
                                          (u16*)d_out, (char*)d_ws);
}
```

```cpp
#include <hip/hip_runtime.h>
#include <stdint.h>

typedef unsigned short u16;
typedef unsigned int u32;
typedef unsigned long long u64;
typedef __attribute__((ext_vector_type(2))) u32 u32x2;
typedef __attribute__((ext_vector_type(4))) u32 u32x4;
typedef __attribute__((ext_vector_type(2))) float f32x2;
typedef __attribute__((ext_vector_type(4))) float f32x4;
typedef __attribute__((ext_vector_type(2))) __bf16 bf16x2;
typedef __attribute__((ext_vector_type(8))) __bf16 bf16x8;
typedef __amdgpu_buffer_rsrc_t rsrc_t;

constexpr int NB = 4, D = 512, H = 32, DH = 128, SK = 4096, W = 1024, F = 1376;
constexpr int NWG = 256, NRED = 32, NMLP = 43;

constexpr u32 OFF_E = 0;
constexpr u32 OFF_FLAG = 1024;
constexpr u32 OFF_H = 4096;
constexpr u32 OFF_O = 8192;
constexpr u32 O_STRIDE = 640;
constexpr u32 OFF_Q = OFF_O + NWG * O_STRIDE;
constexpr u32 OFF_Y = OFF_Q + NWG * 2048;
constexpr u32 OFF_D = OFF_Y + NWG * 2048;
constexpr u32 WS_USED = OFF_D + NMLP * 8192;
constexpr u32 OFF_TS = 2u << 20;

constexpr u32 SENT = 0xFFFFFFFFu;
constexpr int AUX_NT = 2, AUX_SC1 = 16;
constexpr u32 SPIN_CAP = 1u << 18;

__device__ __forceinline__ rsrc_t mk_rsrc(const void* p, u32 bytes) {
    return __builtin_amdgcn_make_buffer_rsrc((void*)p, 0, (int)bytes, 0x00020000);
}
#define WQ_AUX 2
#define WO_AUX 2
template <int AUX> __device__ __forceinline__ u32x4 ldx(rsrc_t r, u32 voff, u32 soff) { return __builtin_amdgcn_raw_buffer_load_b128(r, voff, soff, AUX); }
__device__ __forceinline__ u32x4 ld16(rsrc_t r, u32 voff, u32 soff, const int aux_nt) {
    return aux_nt ? __builtin_amdgcn_raw_buffer_load_b128(r, voff, soff, AUX_NT)
                  : __builtin_amdgcn_raw_buffer_load_b128(r, voff, soff, 0);
}
__device__ __forceinline__ u32x4 ld16_sc1(rsrc_t r, u32 voff) { return __builtin_amdgcn_raw_buffer_load_b128(r, voff, 0, AUX_SC1); }
__device__ __forceinline__ u32x2 ld8_sc1(rsrc_t r, u32 voff) { return __builtin_amdgcn_raw_buffer_load_b64(r, voff, 0, AUX_SC1); }
__device__ __forceinline__ void st16_sc1(u32x4 v, rsrc_t r, u32 voff) { __builtin_amdgcn_raw_buffer_store_b128(v, r, voff, 0, AUX_SC1); }
__device__ __forceinline__ void st8_sc1(u32x2 v, rsrc_t r, u32 voff) { __builtin_amdgcn_raw_buffer_store_b64(v, r, voff, 0, AUX_SC1); }

__device__ __forceinline__ float bf_lo(u32 v) { return __uint_as_float(v << 16); }
__device__ __forceinline__ float bf_hi(u32 v) { return __uint_as_float(v & 0xffff0000u); }
__device__ __forceinline__ u32 pk_bf16(float a, float b) {
    f32x2 v = {a, b};
    return __builtin_bit_cast(u32, __builtin_convertvector(v, bf16x2));
}
__device__ __forceinline__ float bfr(float a) { return bf_lo(pk_bf16(a, a)); }
__device__ __forceinline__ u32 canon(u32 v) { return v < SENT ? v : SENT - 1; }
__device__ __forceinline__ u32x4 canon4(f32x4 v) {
    u32x4 r;
#pragma unroll
    for (int i = 0; i < 4; ++i) r[i] = canon(__float_as_uint(v[i]));
    return r;
}
__device__ __forceinline__ u32 umax(u32 a, u32 b) { return a > b ? a : b; }
__device__ __forceinline__ u32 umax4(u32x4 v) { return umax(umax(v[0], v[1]), umax(v[2], v[3])); }
__device__ __forceinline__ bool full4(u32x4 v) { return umax4(v) != SENT; }
typedef __attribute__((address_space(3))) const u32 lds_cu32;
__device__ __forceinline__ u32x4 lds_poll16(const u32* p) {
    u32x4 v;
    asm volatile("ds_read_b128 %0, %1\n\ts_waitcnt lgkmcnt(0)" : "=v"(v) : "v"((u32)(size_t)(lds_cu32*)p) : "memory");
    return v;
}
__device__ __forceinline__ void keep(bf16x8& v) {
    u32x4 r = __builtin_bit_cast(u32x4, v);
    asm volatile("" : "+v"(r));
    v = __builtin_bit_cast(bf16x8, r);
}

template <int CTRL> __device__ __forceinline__ float dpp(float v) {
    return __uint_as_float(__builtin_amdgcn_update_dpp(0u, __float_as_uint(v), CTRL, 0xf, 0xf, true));
}
template <int CTRL> __device__ __forceinline__ u32 dppu(u32 v) { return __builtin_amdgcn_update_dpp(0u, v, CTRL, 0xf, 0xf, true); }
constexpr int DPP_X1 = 0xB1, DPP_X2 = 0x4E, DPP_HMIR = 0x141, DPP_MIR = 0x140;
constexpr int DPP_ROR8 = 0x128;
constexpr int DPP_SHL1 = 0x101, DPP_SHL2 = 0x102, DPP_SHL4 = 0x104, DPP_SHL6 = 0x106;

__device__ __forceinline__ float row8_sum(float v) { v += dpp<DPP_X1>(v); v += dpp<DPP_X2>(v); v += dpp<DPP_HMIR>(v); return v; }
__device__ __forceinline__ float row16_sum(float v) { v = row8_sum(v); v += dpp<DPP_MIR>(v); return v; }
__device__ __forceinline__ float row16_max(float v) {
    v = fmaxf(v, dpp<DPP_X1>(v)); v = fmaxf(v, dpp<DPP_X2>(v));
    v = fmaxf(v, dpp<DPP_HMIR>(v)); v = fmaxf(v, dpp<DPP_MIR>(v));
    return v;
}
__device__ __forceinline__ float xrow_sum(float x) {
    auto s = __builtin_amdgcn_permlane16_swap(__float_as_uint(x), __float_as_uint(x), false, false);
    x = __uint_as_float(s[0]) + __uint_as_float(s[1]);
    auto t = __builtin_amdgcn_permlane32_swap(__float_as_uint(x), __float_as_uint(x), false, false);
    return __uint_as_float(t[0]) + __uint_as_float(t[1]);
}
__device__ __forceinline__ float xrow_max(float x) {
    auto s = __builtin_amdgcn_permlane16_swap(__float_as_uint(x), __float_as_uint(x), false, false);
    x = fmaxf(__uint_as_float(s[0]), __uint_as_float(s[1]));
    auto t = __builtin_amdgcn_permlane32_swap(__float_as_uint(x), __float_as_uint(x), false, false);
    return fmaxf(__uint_as_float(t[0]), __uint_as_float(t[1]));
}
__device__ __forceinline__ float wave_sum(float v) { return xrow_sum(row16_sum(v)); }

__device__ __forceinline__ void lds_barrier() { asm volatile("s_waitcnt lgkmcnt(0)\n\ts_barrier" ::: "memory"); }
__device__ __forceinline__ void pin() { __builtin_amdgcn_sched_barrier(0); }

__device__ __forceinline__ void unpack8(u32x4 v, float* f) {
#pragma unroll
    for (int j = 0; j < 4; ++j) { f[2 * j] = bf_lo(v[j]); f[2 * j + 1] = bf_hi(v[j]); }
}

__device__ __forceinline__ void ln_stats(u32x4 row, float& mb, float& rs) {
    float f[8];
    unpack8(row, f);
    float s = ((f[0] + f[1]) + (f[2] + f[3])) + ((f[4] + f[5]) + (f[6] + f[7]));
    float mean = wave_sum(s) * (1.f / D);
    float q = 0.f;
#pragma unroll
    for (int i = 0; i < 8; ++i) { float d = f[i] - mean; q += d * d; }
    float var = wave_sum(q) * (1.f / D);
    mb = bfr(mean);
    float sq = bfr(__builtin_amdgcn_sqrtf(bfr(bfr(var) + 1e-5f)));
    rs = __builtin_amdgcn_rcpf(sq);
}
__device__ __forceinline__ u32x4 ln_apply(u32x4 v, float mb, float rs) {
    u32x4 r;
#pragma unroll
    for (int j = 0; j < 4; ++j) {
        u32 d = pk_bf16(bf_lo(v[j]) - mb, bf_hi(v[j]) - mb);
        r[j] = pk_bf16(bf_lo(d) * rs, bf_hi(d) * rs);
    }
    return r;
}

__device__ __forceinline__ bf16x8 colfrag(const u32x4* in, int c) {
    const u32 sel = (c & 1) ? 0x07060302u : 0x05040100u;
    u32x4 r;
#pragma unroll
    for (int q = 0; q < 4; ++q) r[q] = __builtin_amdgcn_perm(in[2 * q + 1][c >> 1], in[2 * q][c >> 1], sel);
    return __builtin_bit_cast(bf16x8, r);
}
__device__ __forceinline__ float dot2(u32 a, u32 b, float c) {
    return __builtin_amdgcn_fdot2_f32_bf16(__builtin_bit_cast(bf16x2, a), __builtin_bit_cast(bf16x2, b), c, false);
}
__device__ __forceinline__ f32x4 mfma(bf16x8 a, bf16x8 b, f32x4 c) { return __builtin_amdgcn_mfma_f32_16x16x32_bf16(a, b, c, 0, 0, 0); }
__device__ __forceinline__ bf16x8 asfrag(u32x4 v) { return __builtin_bit_cast(bf16x8, v); }

#define STAMP(k) do { } while (0)
#define STAMP_INIT do { } while (0)
#define STAMP_FLUSH do { } while (0)

#define PREF 1
#define KPRE 2
#define KMID 5

__global__ __launch_bounds__(1024) void fused_block(const u16* __restrict__ x, const u16* __restrict__ kc, const u16* __restrict__ vc,
                                                    const u16* __restrict__ wq, const u16* __restrict__ wo, const u16* __restrict__ wg_,
                                                    const u16* __restrict__ wu, const u16* __restrict__ wd, u16* __restrict__ out,
                                                    char* __restrict__ ws) {
    __shared__ __attribute__((aligned(16))) u32 s_qb[64];
    __shared__ __attribute__((aligned(16))) float s_o[16 * 132];
    __shared__ __attribute__((aligned(16))) f32x2 s_ml[16];
    __shared__ __attribute__((aligned(16))) float s_r[64 * 20];
    __shared__ __attribute__((aligned(16))) float s_r2[64 * 20];
    __shared__ __attribute__((aligned(16))) u16 s_hn[4 * 520];
    __shared__ __attribute__((aligned(16))) float s_gu[2 * 16 * 32 * 4];
    __shared__ __attribute__((aligned(16))) u16 s_a[4 * 32];

    const int t = threadIdx.x, l = t & 63, g = l >> 4, n = l & 15;
    const int w = __builtin_amdgcn_readfirstlane(t >> 6);
    const int wg = blockIdx.x;
    const int h = (wg & 7) * 4 + (wg >> 6), jj = (wg >> 3) & 7, b = jj >> 1, half = jj & 1;
    const int bh = b * H + h, gi = h * 8 + jj;
    const bool is_red = wg < NRED, is_mlp = wg >= NRED && wg < NRED + NMLP;
    const int mx7 = wg & 7;
    const int mj = mx7 * 5 + (mx7 < 3 ? mx7 : 3) + ((wg - NRED) >> 3);

    const rsrc_t r_ws = mk_rsrc(ws, WS_USED);
    STAMP_INIT;
    STAMP(0);

    const size_t kvbase = ((size_t)bh * SK + (SK - W) + half * 512) * DH;
    const u32 kvo = w * 8192 + l * 16;
    const rsrc_t r_k = mk_rsrc(kc + kvbase, 512 * DH * 2);
    const rsrc_t r_v = mk_rsrc(vc + kvbase, 512 * DH * 2);
    u32x4 kv[8], vv[8];
    u32 tg0 = 0, tg1 = 0;

    if (w == 0) {
        __builtin_amdgcn_s_setprio(3);
        s_qb[l] = SENT;
        const u32x2 ev = ld8_sc1(r_ws, OFF_E);
        pin();
        {
            const u32x4 s4 = {SENT, SENT, SENT, SENT};
            st16_sc1(s4, r_ws, OFF_Q + gi * 2048 + l * 16);
            st16_sc1(s4, r_ws, OFF_Q + gi * 2048 + 1024 + l * 16);
            if (l < 33) st16_sc1(s4, r_ws, OFF_O + gi * O_STRIDE + l * 16);
            st16_sc1(s4, r_ws, OFF_Y + gi * 2048 + l * 16);
            st16_sc1(s4, r_ws, OFF_Y + gi * 2048 + 1024 + l * 16);
            if (is_red && l < 8) st16_sc1(s4, r_ws, OFF_H + wg * 128 + l * 16);
            if (is_mlp) {
#pragma unroll
                for (int i = 0; i < 8; ++i) st16_sc1(s4, r_ws, OFF_D + mj * 8192 + i * 1024 + l * 16);
            }
        }
        asm volatile("" ::: "memory");
        pin();
        const rsrc_t r_x = mk_rsrc(x, NB * D * 2);
        u32x4 xs[4], xf[2], wv[16];
#pragma unroll
        for (int i = 0; i < 4; ++i) xs[i] = ld16(r_x, (n >> 2) * 1024 + ((n & 3) * 4 + g) * 64 + i * 16, 0, 0);
#pragma unroll
        for (int s = 0; s < 2; ++s) xf[s] = ld16(r_x, (n >> 2) * 1024 + jj * 128 + s * 64 + g * 16, 0, 0);
        {
            const rsrc_t r = mk_rsrc(wq + h * DH, (D * H * DH - h * DH) * 2);
            const u32 o = (u32)(jj * 64 + g * 8) * 8192u + n * 16;
#pragma unroll
            for (int i = 0; i < 16; ++i) wv[i] = ldx<WQ_AUX>(r, o, ((i >> 3) * 32 + (i & 7)) * 8192);
        }
        pin();
        asm volatile("s_waitcnt vmcnt(22)" ::: "memory");
        pin();
        {
            const u64 e = ((u64)(u32)__builtin_amdgcn_readfirstlane(ev[1]) << 32) | (u32)__builtin_amdgcn_readfirstlane(ev[0]);
            const u64 tag = e * 6364136223846793005ull + 1442695040888963407ull;
            tg0 = (u32)tag; tg1 = (u32)(tag >> 32);
        }
        if (l == 0) st8_sc1(u32x2{tg0, tg1}, r_ws, OFF_FLAG + gi * 8);
        pin();
        lds_barrier();
        pin();
        STAMP(2);
        float f[32];
#pragma unroll
        for (int i = 0; i < 4; ++i) unpack8(xs[i], f + 8 * i);
        float s0 = 0.f, s1 = 0.f, s2 = 0.f, s3 = 0.f;
#pragma unroll
        for (int i = 0; i < 8; ++i) { s0 += f[i]; s1 += f[8 + i]; s2 += f[16 + i]; s3 += f[24 + i]; }
        float sm = (s0 + s1) + (s2 + s3);
        sm += dpp<DPP_X1>(sm); sm += dpp<DPP_X2>(sm);
        const float mean = xrow_sum(sm) * (1.f / D);
        s0 = s1 = s2 = s3 = 0.f;
#pragma unroll
        for (int i = 0; i < 8; ++i) {
            const float d0 = f[i] - mean, d1 = f[8 + i] - mean, d2 = f[16 + i] - mean, d3 = f[24 + i] - mean;
            s0 += d0 * d0; s1 += d1 * d1; s2 += d2 * d2; s3 += d3 * d3;
        }
        float sq = (s0 + s1) + (s2 + s3);
        sq += dpp<DPP_X1>(sq); sq += dpp<DPP_X2>(sq);
        const float var = xrow_sum(sq) * (1.f / D);
        const float mb = bfr(mean);
        const float rs = __builtin_amdgcn_rcpf(bfr(__builtin_amdgcn_sqrtf(bfr(bfr(var) + 1e-5f))));
        bf16x8 xa0 = asfrag(ln_apply(xf[0], mb, rs)), xa1 = asfrag(ln_apply(xf[1], mb, rs));
        keep(xa0); keep(xa1);
        pin();
        u32x4 fl = ld16_sc1(r_ws, OFF_FLAG + h * 64 + (l & 3) * 16);
        pin();
        STAMP(1);
        float qp[8];
#pragma unroll
        for (int c = 0; c < 8; ++c) {
            f32x4 a = mfma(xa0, colfrag(wv, c), f32x4{0.f, 0.f, 0.f, 0.f});
            qp[c] = mfma(xa1, colfrag(wv + 8, c), a)[0];
        }
        STAMP(9);
        const u32 qo = OFF_Q + gi * 2048 + l * 16;
        st16_sc1(canon4(f32x4{qp[0], qp[1], qp[2], qp[3]}), r_ws, qo);
        st16_sc1(canon4(f32x4{qp[4], qp[5], qp[6], qp[7]}), r_ws, qo + 1024);
        pin();
        asm volatile("s_barrier" ::: "memory");
        pin();
        STAMP(10);
        for (u32 spin = 0; spin < SPIN_CAP; ++spin) {
            if (__all(((fl[0] ^ tg0) | (fl[1] ^ tg1) | (fl[2] ^ tg0) | (fl[3] ^ tg1)) == 0)) break;
            asm volatile("" ::: "memory");
            fl = ld16_sc1(r_ws, OFF_FLAG + h * 64 + (l & 3) * 16);
        }
        asm volatile("" ::: "memory");
        STAMP(3);
        {
            const u32 o = OFF_Q + (u32)(h * 8 + (g >> 1)) * 2048u + (g & 1) * 1024 + (b * 16 + n) * 16;
            u32x4 qv[4];
            for (u32 spin = 0; spin < SPIN_CAP; ++spin) {
                asm volatile("" ::: "memory");
#pragma unroll
                for (int i = 0; i < 4; ++i) qv[i] = ld16_sc1(r_ws, o + i * 4096);
                if (__all(umax(umax(umax4(qv[0]), umax4(qv[1])), umax(umax4(qv[2]), umax4(qv[3]))) != SENT)) break;
            }
            float q4[4];
#pragma unroll
            for (int j = 0; j < 4; ++j) {
                const float v = (__uint_as_float(qv[0][j]) + __uint_as_float(qv[1][j])) + (__uint_as_float(qv[2][j]) + __uint_as_float(qv[3][j]));
                auto sw = __builtin_amdgcn_permlane32_swap(__float_as_uint(v), __float_as_uint(v), false, false);
                q4[j] = __uint_as_float(sw[0]) + __uint_as_float(sw[1]);
            }
            if (l < 32) *(u32x2*)&s_qb[n * 4 + g * 2] = u32x2{canon(pk_bf16(q4[0], q4[1])), canon(pk_bf16(q4[2], q4[3]))};
        }
        pin();
#pragma unroll
        for (int i = 0; i < KMID; ++i) {
            if (i < 8) kv[i & 7] = ld16(r_k, kvo + (i & 3) * 1024, ((i >> 2) & 1) * 4096, 1);
            else vv[i & 7] = ld16(r_v, kvo + (i & 3) * 1024, ((i >> 2) & 1) * 4096, 1);
        }
    } else {
#pragma unroll
        for (int i = 0; i < KPRE; ++i) kv[i] = ld16(r_k, kvo + (i & 3) * 1024, (i >> 2) * 4096, 1);
        pin();
        asm volatile("s_barrier" ::: "memory");
        pin();
#pragma unroll
        for (int i = KPRE; i < KMID; ++i) {
            if (i < 8) kv[i & 7] = ld16(r_k, kvo + (i & 3) * 1024, ((i >> 2) & 1) * 4096, 1);
            else vv[i & 7] = ld16(r_v, kvo + (i & 3) * 1024, ((i >> 2) & 1) * 4096, 1);
        }
        pin();
        asm volatile("s_barrier" ::: "memory");
    }
    pin();
#pragma unroll
    for (int i = KMID; i < 16; ++i) {
        if (i < 8) kv[i & 7] = ld16(r_k, kvo + (i & 3) * 1024, ((i >> 2) & 1) * 4096, 1);
        else vv[i & 7] = ld16(r_v, kvo + (i & 3) * 1024, ((i >> 2) & 1) * 4096, 1);
    }
    u32x4 fl0 = {0, 0, 0, 0}, fl1 = {0, 0, 0, 0};
    const bool chk = (w == 0) && (is_red || is_mlp);
    if (chk) { fl0 = ld16_sc1(r_ws, OFF_FLAG + l * 16); fl1 = ld16_sc1(r_ws, OFF_FLAG + 1024 + l * 16); }
    pin();
    STAMP(4);

    u32x4 qf;
    for (u32 spin = 0; spin < SPIN_CAP; ++spin) {
        qf = lds_poll16(&s_qb[n * 4]);
        if (__all(full4(qf))) break;
        __builtin_amdgcn_s_sleep(2);
    }
    STAMP(5);

    float sc[8];
#pragma unroll
    for (int i = 0; i < 8; ++i) {
        float a = 0.f;
#pragma unroll
        for (int j = 0; j < 4; ++j)
            a = dot2(kv[i][j], qf[j], a);
        sc[i] = row16_sum(a);
    }
#pragma unroll
    for (int i = 0; i < 8; i += 2) {
        u32 p = pk_bf16(sc[i], sc[i + 1]);
        p = pk_bf16(bf_lo(p) * 0.08838834764831845f, bf_hi(p) * 0.08838834764831845f);
        sc[i] = bf_lo(p); sc[i + 1] = bf_hi(p);
    }
    float mx = fmaxf(fmaxf(fmaxf(sc[0], sc[1]), fmaxf(sc[2], sc[3])), fmaxf(fmaxf(sc[4], sc[5]), fmaxf(sc[6], sc[7])));
    mx = xrow_max(mx);
    u32x4 pa;
    float ls = 0.f;
#pragma unroll
    for (int i = 0; i < 8; i += 2) {
        const float p0 = __builtin_amdgcn_exp2f((sc[i] - mx) * 1.4426950408889634f);
        const float p1 = __builtin_amdgcn_exp2f((sc[i + 1] - mx) * 1.4426950408889634f);
        ls += p0 + p1;
        pa[i >> 1] = pk_bf16(p0, p1);
    }
    ls = xrow_sum(ls);
    if (l == 0) s_ml[w] = f32x2{mx, ls};
    pin();
    STAMP(6);

    {
        float op[8];
#pragma unroll
        for (int c = 0; c < 8; ++c) op[c] = mfma(asfrag(pa), colfrag(vv, c), f32x4{0.f, 0.f, 0.f, 0.f})[0];
        if (l < 16) {
            *(f32x4*)&s_o[w * 132 + n * 8] = f32x4{op[0], op[1], op[2], op[3]};
            *(f32x4*)&s_o[w * 132 + n * 8 + 4] = f32x4{op[4], op[5], op[6], op[7]};
        }
    }
    pin();
    STAMP(7);
    const int kh = jj >> 2, cq = jj & 3;
    u32x4 wb[16];
    if (w == 1) {
        const rsrc_t r = mk_rsrc(wo + (size_t)(h * DH + kh * 64) * D + cq * 128, (64 * D - cq * 128) * 2);
        const u32 o = (u32)g * 8192u + n * 16;
#pragma unroll
        for (int i = 0; i < 16; ++i) wb[i] = ldx<WO_AUX>(r, o + (i & 3) * 1024, (i >> 2) * 4096 + (i >> 3) * 24576);
    }
    if (chk) {
        for (u32 spin = 0; spin < SPIN_CAP; ++spin) {
            const bool ok = ((fl0[0] ^ tg0) | (fl0[1] ^ tg1) | (fl0[2] ^ tg0) | (fl0[3] ^ tg1) |
                             (fl1[0] ^ tg0) | (fl1[1] ^ tg1) | (fl1[2] ^ tg0) | (fl1[3] ^ tg1)) == 0;
            if (__all(ok)) break;
            asm volatile("" ::: "memory");
            fl0 = ld16_sc1(r_ws, OFF_FLAG + l * 16); fl1 = ld16_sc1(r_ws, OFF_FLAG + 1024 + l * 16);
        }
        if (wg == 0 && l == 0) st8_sc1(u32x2{tg0, tg1}, r_ws, OFF_E);
    }
    pin();
    lds_barrier();
    STAMP(8);

    u32 pv = 0, pv2 = 0;
    if (w >= 4 && w < 8) {
        const int u = w - 4;
        const f32x2 ml = s_ml[n];
        const f32x4 a = *(const f32x4*)&s_o[n * 132 + u * 32 + g * 8], a2 = *(const f32x4*)&s_o[n * 132 + u * 32 + g * 8 + 4];
        const float M = row16_max(ml[0]);
        const float f = __builtin_amdgcn_exp2f((ml[0] - M) * 1.4426950408889634f);
        f32x4 v, v2;
#pragma unroll
        for (int i = 0; i < 4; ++i) { v[i] = row16_sum(a[i] * f); v2[i] = row16_sum(a2[i] * f); }
        const float L = row16_sum(ml[1] * f);
        if (n < 2) st16_sc1(canon4(n ? v2 : v), r_ws, OFF_O + gi * O_STRIDE + (u * 32 + g * 8 + n * 4) * 4);
        if (u == 0 && l == 0) st8_sc1(u32x2{canon(__float_as_uint(M)), canon(__float_as_uint(L))}, r_ws, OFF_O + gi * O_STRIDE + 512);
        STAMP(9);
        if (PREF) {
            pin();
            const int uu = wg >> 3, m0 = mx7 * 5 + (mx7 < 3 ? mx7 : 3), nm = mx7 < 3 ? 6 : 5;
            {
                const int seg = l & 7, row = uu * 16 + (u >> 1) * 8 + (l >> 3);
                const rsrc_t r = mk_rsrc((u & 1) ? wu : wg_, D * F * 2);
                if (seg < nm) pv = __builtin_amdgcn_raw_buffer_load_b32(r, (u32)row * (F * 2) + (m0 + seg) * 64, 0, 0);
            }
            if (u < 2) {
                const int row = uu * nm + u * 4 + g;
                const rsrc_t r = mk_rsrc(wd, F * D * 2);
                if (u * 4 + g < nm) pv2 = __builtin_amdgcn_raw_buffer_load_b32(r, (u32)(m0 * 32 + row) * 1024u + n * 64, 0, 0);
            }
        }
    } else if (w == 1) {
        __builtin_amdgcn_s_setprio(3);
        bf16x8 b0[8], b1[8];
#pragma unroll
        for (int c = 0; c < 8; ++c) { b0[c] = colfrag(wb, c); b1[c] = colfrag(wb + 8, c); keep(b0[c]); keep(b1[c]); }
        pin();
        const u32 so = OFF_O + (u32)(h * 8 + (n >> 2) * 2 + (n & 1)) * O_STRIDE;
        const u32 oo = so + (kh * 64 + ((n >> 1) & 1) * 32 + g * 8) * 4;
        u32x4 o0, o1;
        u32x2 ml;
        for (u32 spin = 0; spin < SPIN_CAP; ++spin) {
            asm volatile("" ::: "memory");
            o0 = ld16_sc1(r_ws, oo); o1 = ld16_sc1(r_ws, oo + 16); ml = ld8_sc1(r_ws, so + 512);
            if (__all(umax(umax(umax4(o0), umax4(o1)), umax(ml[0], ml[1])) != SENT)) break;
        }
        STAMP(9);
        const float m = __uint_as_float(ml[0]);
        const float M = fmaxf(m, dpp<DPP_X1>(m));
        const float e = __builtin_amdgcn_exp2f((m - M) * 1.4426950408889634f);
        const float le = __uint_as_float(ml[1]) * e;
        const float wt = e * __builtin_amdgcn_rcpf(le + dpp<DPP_X1>(le));
        float v[8];
#pragma unroll
        for (int i = 0; i < 4; ++i) { v[i] = __uint_as_float(o0[i]) * wt; v[4 + i] = __uint_as_float(o1[i]) * wt; }
#pragma unroll
        for (int i = 0; i < 8; ++i) v[i] += dpp<DPP_X1>(v[i]);
        const bf16x8 oa = asfrag(u32x4{pk_bf16(v[0], v[1]), pk_bf16(v[2], v[3]), pk_bf16(v[4], v[5]), pk_bf16(v[6], v[7])});
        float yp[8];
#pragma unroll
        for (int c = 0; c < 8; ++c)
            yp[c] = mfma(oa, b0[c], f32x4{0.f, 0.f, 0.f, 0.f})[0] + mfma(oa, b1[c], f32x4{0.f, 0.f, 0.f, 0.f})[2];
        const u32 yo = OFF_Y + gi * 2048 + l * 16;
        st16_sc1(canon4(f32x4{yp[0], yp[1], yp[2], yp[3]}), r_ws, yo);
        st16_sc1(canon4(f32x4{yp[4], yp[5], yp[6], yp[7]}), r_ws, yo + 1024);
        STAMP(10);
    }
    if (is_red || is_mlp) lds_barrier();
    pin();

    if (is_red) {
        const int rb = wg >> 3, ch = wg & 7;
        float xres = 0.f;
        if (w == 0) xres = __uint_as_float((u32)x[rb * D + ch * 64 + l] << 16);
        const int p = w * 4 + g;
        const u32 sub = (n >> 3) * 1024 + rb * 256 + (ch & 1) * 128 + (n & 7) * 16;
        const u32 yo = OFF_Y + (u32)((p >> 1) * 8 + (p & 1) * 4 + (ch >> 1)) * 2048u + sub;
        u32x4 yv;
        for (u32 spin = 0; spin < SPIN_CAP; ++spin) {
            asm volatile("" ::: "memory");
            yv = ld16_sc1(r_ws, yo);
            if (__all(full4(yv))) break;
        }
        STAMP(11);
        {
            float v[4];
#pragma unroll
            for (int i = 0; i < 4; ++i) v[i] = xrow_sum(__uint_as_float(yv[i]));
            if (l < 16) {
#pragma unroll
                for (int i = 0; i < 4; ++i) s_r[((n & 7) * 8 + (n >> 3) * 4 + i) * 20 + w] = v[i];
            }
        }
        lds_barrier();
        float hval = 0.f;
        if (w == 0) {
            const f32x4 a = *(const f32x4*)&s_r[l * 20], bb = *(const f32x4*)&s_r[l * 20 + 4];
            const f32x4 c = *(const f32x4*)&s_r[l * 20 + 8], d = *(const f32x4*)&s_r[l * 20 + 12];
            const f32x4 e = (a + bb) + (c + d);
            hval = bfr(xres + bfr((e[0] + e[1]) + (e[2] + e[3])));
            const u32 pr = __builtin_amdgcn_perm(dppu<DPP_SHL1>(__float_as_uint(hval)), __float_as_uint(hval), 0x07060302u);
            const u32x4 hv = {canon(pr), canon(dppu<DPP_SHL2>(pr)), canon(dppu<DPP_SHL4>(pr)), canon(dppu<DPP_SHL6>(pr))};
            if ((l & 7) == 0) st16_sc1(hv, r_ws, OFF_H + wg * 128 + l * 2);
        }
        STAMP(12);
        if (w < 11) {
            const int pj = w * 4 + g;
            const int pjc = pj < NMLP ? pj : NMLP - 1;
            const u32 o = OFF_D + (u32)pjc * 8192u + (ch >> 1) * 2048 + sub;
            u32x4 dv;
            for (u32 spin = 0; spin < SPIN_CAP; ++spin) {
                asm volatile("" ::: "memory");
                dv = ld16_sc1(r_ws, o);
                if (__all(full4(dv))) break;
            }
            STAMP(13);
            float v[4];
#pragma unroll
            for (int i = 0; i < 4; ++i) v[i] = xrow_sum(pj < NMLP ? __uint_as_float(dv[i]) : 0.f);
            if (l < 16) {
#pragma unroll
                for (int i = 0; i < 4; ++i) s_r2[((n & 7) * 8 + (n >> 3) * 4 + i) * 20 + w] = v[i];
            }
        }
        lds_barrier();
        if (w == 0) {
            const f32x4 a = *(const f32x4*)&s_r2[l * 20], bb = *(const f32x4*)&s_r2[l * 20 + 4], c = *(const f32x4*)&s_r2[l * 20 + 8];
            const f32x4 e = a + bb;
            const float sum = ((e[0] + e[1]) + (e[2] + e[3])) + ((c[0] + c[1]) + c[2]);
            const float o = hval + bfr(sum);
            out[rb * D + ch * 64 + l] = (u16)pk_bf16(o, o);
        }
        STAMP(14);
    } else if (is_mlp) {
        if (w >= 8) {
            const int mat = w & 1, kq = (w - 8) >> 1, kb = n >> 2, chunk = n & 3;
            const rsrc_t r = mk_rsrc(mat ? wu : wg_, D * F * 2);
            const u32 o = (u32)(kq * 128 + kb * 32 + g * 8) * (F * 2) + mj * 64 + chunk * 16;
            u32x4 w0[8];
#pragma unroll
            for (int i = 0; i < 8; ++i) w0[i] = ld16(r, o, i * (F * 2), 0);
            bf16x8 f0[8];
#pragma unroll
            for (int c = 0; c < 8; ++c) { f0[c] = colfrag(w0, c); keep(f0[c]); }
            pin();
            STAMP(11);
            lds_barrier();
            const int ab = n & 3, akb = n >> 2;
            const bf16x8 a0 = asfrag(*(const u32x4*)&s_hn[ab * 520 + kq * 128 + akb * 32 + g * 8]);
            f32x4 acc[8];
#pragma unroll
            for (int c = 0; c < 8; ++c) acc[c] = mfma(a0, f0[c], f32x4{0.f, 0.f, 0.f, 0.f});
            if (kb == g) {
#pragma unroll
                for (int c = 0; c < 8; ++c) *(f32x4*)&s_gu[((mat * 16 + kq * 4 + kb) * 32 + chunk * 8 + c) * 4] = acc[c];
            }
            lds_barrier();
            lds_barrier();
        } else if (w >= 4) {
            const int c4 = w - 4;
            const rsrc_t r = mk_rsrc(wd + (size_t)mj * 32 * D, 32 * D * 2);
            const u32 o = (u32)(g * 8) * 1024u + c4 * 256 + n * 16;
            u32x4 w0[8];
#pragma unroll
            for (int i = 0; i < 8; ++i) w0[i] = ld16(r, o + (i & 3) * 1024, (i >> 2) * 4096, 0);
            bf16x8 f0[8];
#pragma unroll
            for (int c = 0; c < 8; ++c) { f0[c] = colfrag(w0, c); keep(f0[c]); }
            pin();
            STAMP(11);
            lds_barrier();
            lds_barrier();
            lds_barrier();
            const bf16x8 aa = asfrag(*(const u32x4*)&s_a[(n >> 2) * 32 + g * 8]);
            float dp[8];
#pragma unroll
            for (int c = 0; c < 8; ++c) dp[c] = mfma(aa, f0[c], f32x4{0.f, 0.f, 0.f, 0.f})[0];
            const u32 so = OFF_D + (u32)mj * 8192u + c4 * 2048 + l * 16;
            st16_sc1(canon4(f32x4{dp[0], dp[1], dp[2], dp[3]}), r_ws, so);
            st16_sc1(canon4(f32x4{dp[4], dp[5], dp[6], dp[7]}), r_ws, so + 1024);
            STAMP(14);
        } else {
            const int hb = w;
            u32x4 hv;
            for (u32 spin = 0; spin < SPIN_CAP; ++spin) {
                asm volatile("" ::: "memory");
                hv = ld16_sc1(r_ws, OFF_H + hb * 1024 + l * 16);
                if (__all(full4(hv))) break;
            }
            STAMP(11);
            float mb, rs;
            ln_stats(hv, mb, rs);
            *(u32x4*)&s_hn[hb * 520 + l * 8] = ln_apply(hv, mb, rs);
            STAMP(12);
            lds_barrier();
            lds_barrier();
            {
                const int col = w * 8 + (n & 7), mat = n >> 3;
                f32x4 s = *(const f32x4*)&s_gu[((mat * 16 + g * 4) * 32 + col) * 4];
#pragma unroll
                for (int i = 1; i < 4; ++i) s += *(const f32x4*)&s_gu[((mat * 16 + g * 4 + i) * 32 + col) * 4];
#pragma unroll
                for (int i = 0; i < 4; ++i) {
                    const float v = bfr(xrow_sum(s[i]));
                    const float pr = v * dpp<DPP_ROR8>(v);
                    if (l < 8) s_a[i * 32 + col] = (u16)pk_bf16(pr, pr);
                }
            }
            STAMP(13);
            lds_barrier();
        }
    }
    asm volatile("" :: "v"(pv), "v"(pv2));
    STAMP(15);
    STAMP_FLUSH;
}

extern "C" void kernel_launch(void* const* d_in, const int* in_sizes, int n_in, void* d_out, int out_size, void* d_ws,
                              size_t ws_size, hipStream_t stream) {
    fused_block<<<NWG, 1024, 0, stream>>>((const u16*)d_in[0], (const u16*)d_in[1], (const u16*)d_in[2], (const u16*)d_in[3],
                                          (const u16*)d_in[4], (const u16*)d_in[6], (const u16*)d_in[7], (const u16*)d_in[8],
                                          (u16*)d_out, (char*)d_ws);
}
```

```cpp
#include <hip/hip_runtime.h>
#include <stdint.h>

typedef unsigned short u16;
typedef unsigned int u32;
typedef unsigned long long u64;
typedef __attribute__((ext_vector_type(2))) u32 u32x2;
typedef __attribute__((ext_vector_type(4))) u32 u32x4;
typedef __attribute__((ext_vector_type(2))) float f32x2;
typedef __attribute__((ext_vector_type(4))) float f32x4;
typedef __attribute__((ext_vector_type(2))) __bf16 bf16x2;
typedef __attribute__((ext_vector_type(8))) __bf16 bf16x8;
typedef __amdgpu_buffer_rsrc_t rsrc_t;

constexpr int NB = 4, D = 512, H = 32, DH = 128, SK = 4096, W = 1024, F = 1376;
constexpr int NWG = 256, NRED = 32, NMLP = 43;

constexpr u32 OFF_E = 0;
constexpr u32 OFF_FLAG = 1024;
constexpr u32 OFF_H = 4096;
constexpr u32 OFF_O = 8192;
constexpr u32 O_STRIDE = 640;
constexpr u32 OFF_Q = OFF_O + NWG * O_STRIDE;
constexpr u32 OFF_Y = OFF_Q + NWG * 2048;
constexpr u32 OFF_D = OFF_Y + NWG * 2048;
constexpr u32 WS_USED = OFF_D + NMLP * 8192;

constexpr int GU_CH = 40, GU_BLK = 4 * GU_CH;
constexpr u32 SENT = 0xFFFFFFFFu;
constexpr int LD_AUX = 16, ST_AUX = 16;
constexpr int K_AUX = 19, V_AUX = 19;
constexpr int WQ_AUX = 2, WO_AUX = 0, MW_AUX = 2, X_AUX = 0, PF_AUX = 0;
constexpr int KPRE = 1;
constexpr u32 SPIN_CAP = 1u << 18;

__device__ __forceinline__ rsrc_t mk_rsrc(const void* p, u32 bytes) {
    return __builtin_amdgcn_make_buffer_rsrc((void*)p, 0, (int)bytes, 0x00020000);
}
template <int AUX> __device__ __forceinline__ u32x4 ldx(rsrc_t r, u32 voff, u32 soff) { return __builtin_amdgcn_raw_buffer_load_b128(r, voff, soff, AUX); }
__device__ __forceinline__ u32x4 ld16_sc1(rsrc_t r, u32 voff) { return __builtin_amdgcn_raw_buffer_load_b128(r, voff, 0, LD_AUX); }
__device__ __forceinline__ u32x2 ld8_sc1(rsrc_t r, u32 voff) { return __builtin_amdgcn_raw_buffer_load_b64(r, voff, 0, LD_AUX); }
__device__ __forceinline__ void st16_sc1(u32x4 v, rsrc_t r, u32 voff) { __builtin_amdgcn_raw_buffer_store_b128(v, r, voff, 0, ST_AUX); }
__device__ __forceinline__ void st8_sc1(u32x2 v, rsrc_t r, u32 voff) { __builtin_amdgcn_raw_buffer_store_b64(v, r, voff, 0, ST_AUX); }

__device__ __forceinline__ float bf_lo(u32 v) { return __uint_as_float(v << 16); }
__device__ __forceinline__ float bf_hi(u32 v) { return __uint_as_float(v & 0xffff0000u); }
__device__ __forceinline__ u32 pk_bf16(float a, float b) {
    f32x2 v = {a, b};
    return __builtin_bit_cast(u32, __builtin_convertvector(v, bf16x2));
}
__device__ __forceinline__ float bfr(float a) { return bf_lo(pk_bf16(a, a)); }
__device__ __forceinline__ u32 canon(u32 v) { return v < SENT ? v : SENT - 1; }
__device__ __forceinline__ u32x4 canon4(f32x4 v) {
    u32x4 r;
#pragma unroll
    for (int i = 0; i < 4; ++i) r[i] = canon(__float_as_uint(v[i]));
    return r;
}
__device__ __forceinline__ u32 umax(u32 a, u32 b) { return a > b ? a : b; }
__device__ __forceinline__ u32 umax4(u32x4 v) { return umax(umax(v[0], v[1]), umax(v[2], v[3])); }
__device__ __forceinline__ bool full4(u32x4 v) { return umax4(v) != SENT; }
typedef __attribute__((address_space(3))) const u32 lds_cu32;
__device__ __forceinline__ u32x4 lds_poll16(const u32* p) {
    u32x4 v;
    asm volatile("ds_read_b128 %0, %1\n\ts_waitcnt lgkmcnt(0)" : "=v"(v) : "v"((u32)(size_t)(lds_cu32*)p) : "memory");
    return v;
}
__device__ __forceinline__ void keep(bf16x8& v) {
    u32x4 r = __builtin_bit_cast(u32x4, v);
    asm volatile("" : "+v"(r));
    v = __builtin_bit_cast(bf16x8, r);
}

template <int CTRL> __device__ __forceinline__ float dpp(float v) {
    return __uint_as_float(__builtin_amdgcn_update_dpp(0u, __float_as_uint(v), CTRL, 0xf, 0xf, true));
}
template <int CTRL> __device__ __forceinline__ u32 dppu(u32 v) { return __builtin_amdgcn_update_dpp(0u, v, CTRL, 0xf, 0xf, true); }
constexpr int DPP_X1 = 0xB1, DPP_X2 = 0x4E, DPP_HMIR = 0x141, DPP_MIR = 0x140;
constexpr int DPP_ROR8 = 0x128;
constexpr int DPP_SHL1 = 0x101, DPP_SHL2 = 0x102, DPP_SHL4 = 0x104, DPP_SHL6 = 0x106;

template <int J> __device__ __forceinline__ float kb_sum(f32x4 a) {
    float r = a[J];
#pragma unroll
    for (int kb = 0; kb < 4; ++kb) {
        if (kb == 0 && J == 1) r += dpp<0x114>(a[0]);
        if (kb == 0 && J == 2) r += dpp<0x118>(a[0]);
        if (kb == 0 && J == 3) r += dpp<0x11C>(a[0]);
        if (kb == 1 && J == 0) r += dpp<0x104>(a[1]);
        if (kb == 1 && J == 2) r += dpp<0x114>(a[1]);
        if (kb == 1 && J == 3) r += dpp<0x118>(a[1]);
        if (kb == 2 && J == 0) r += dpp<0x108>(a[2]);
        if (kb == 2 && J == 1) r += dpp<0x104>(a[2]);
        if (kb == 2 && J == 3) r += dpp<0x114>(a[2]);
        if (kb == 3 && J == 0) r += dpp<0x10C>(a[3]);
        if (kb == 3 && J == 1) r += dpp<0x108>(a[3]);
        if (kb == 3 && J == 2) r += dpp<0x104>(a[3]);
    }
    return r;
}
template <int J> __device__ __forceinline__ float bank_put(float old, float v) {
    return __uint_as_float(__builtin_amdgcn_update_dpp(__float_as_uint(old), __float_as_uint(v), 0xE4, 0xf, 1 << J, false));
}
__device__ __forceinline__ float row8_sum(float v) { v += dpp<DPP_X1>(v); v += dpp<DPP_X2>(v); v += dpp<DPP_HMIR>(v); return v; }
__device__ __forceinline__ float row16_sum(float v) { v = row8_sum(v); v += dpp<DPP_MIR>(v); return v; }
__device__ __forceinline__ float row16_max(float v) {
    v = fmaxf(v, dpp<DPP_X1>(v)); v = fmaxf(v, dpp<DPP_X2>(v));
    v = fmaxf(v, dpp<DPP_HMIR>(v)); v = fmaxf(v, dpp<DPP_MIR>(v));
    return v;
}
__device__ __forceinline__ float xrow_sum(float x) {
    auto s = __builtin_amdgcn_permlane16_swap(__float_as_uint(x), __float_as_uint(x), false, false);
    x = __uint_as_float(s[0]) + __uint_as_float(s[1]);
    auto t = __builtin_amdgcn_permlane32_swap(__float_as_uint(x), __float_as_uint(x), false, false);
    return __uint_as_float(t[0]) + __uint_as_float(t[1]);
}
__device__ __forceinline__ float xrow_max(float x) {
    auto s = __builtin_amdgcn_permlane16_swap(__float_as_uint(x), __float_as_uint(x), false, false);
    x = fmaxf(__uint_as_float(s[0]), __uint_as_float(s[1]));
    auto t = __builtin_amdgcn_permlane32_swap(__float_as_uint(x), __float_as_uint(x), false, false);
    return fmaxf(__uint_as_float(t[0]), __uint_as_float(t[1]));
}
__device__ __forceinline__ float wave_sum(float v) { return xrow_sum(row16_sum(v)); }

__device__ __forceinline__ void lds_barrier() { asm volatile("s_waitcnt lgkmcnt(0)\n\ts_barrier" ::: "memory"); }
__device__ __forceinline__ void pin() { __builtin_amdgcn_sched_barrier(0); }

__device__ __forceinline__ void unpack8(u32x4 v, float* f) {
#pragma unroll
    for (int j = 0; j < 4; ++j) { f[2 * j] = bf_lo(v[j]); f[2 * j + 1] = bf_hi(v[j]); }
}

__device__ __forceinline__ void ln_stats(u32x4 row, float& mb, float& rs) {
    float f[8];
    unpack8(row, f);
    float s = ((f[0] + f[1]) + (f[2] + f[3])) + ((f[4] + f[5]) + (f[6] + f[7]));
    float mean = wave_sum(s) * (1.f / D);
    float q = 0.f;
#pragma unroll
    for (int i = 0; i < 8; ++i) { float d = f[i] - mean; q += d * d; }
    float var = wave_sum(q) * (1.f / D);
    mb = bfr(mean);
    float sq = bfr(__builtin_amdgcn_sqrtf(bfr(bfr(var) + 1e-5f)));
    rs = __builtin_amdgcn_rcpf(sq);
}
__device__ __forceinline__ u32x4 ln_apply(u32x4 v, float mb, float rs) {
    u32x4 r;
#pragma unroll
    for (int j = 0; j < 4; ++j) {
        u32 d = pk_bf16(bf_lo(v[j]) - mb, bf_hi(v[j]) - mb);
        r[j] = pk_bf16(bf_lo(d) * rs, bf_hi(d) * rs);
    }
    return r;
}

__device__ __forceinline__ bf16x8 colfrag(const u32x4* in, int c) {
    const u32 sel = (c & 1) ? 0x07060302u : 0x05040100u;
    u32x4 r;
#pragma unroll
    for (int q = 0; q < 4; ++q) r[q] = __builtin_amdgcn_perm(in[2 * q + 1][c >> 1], in[2 * q][c >> 1], sel);
    return __builtin_bit_cast(bf16x8, r);
}
__device__ __forceinline__ float dot2(u32 a, u32 b, float c) {
    return __builtin_amdgcn_fdot2_f32_bf16(__builtin_bit_cast(bf16x2, a), __builtin_bit_cast(bf16x2, b), c, false);
}
__device__ __forceinline__ f32x4 mfma(bf16x8 a, bf16x8 b, f32x4 c) { return __builtin_amdgcn_mfma_f32_16x16x32_bf16(a, b, c, 0, 0, 0); }
__device__ __forceinline__ bf16x8 asfrag(u32x4 v) { return __builtin_bit_cast(bf16x8, v); }

__global__ __launch_bounds__(1024, 4) void fused_block(const u16* __restrict__ x, const u16* __restrict__ kc, const u16* __restrict__ vc,
                                                    const u16* __restrict__ wq, const u16* __restrict__ wo, const u16* __restrict__ wg_,
                                                    const u16* __restrict__ wu, const u16* __restrict__ wd, u16* __restrict__ out,
                                                    char* ws) {
    __shared__ __attribute__((aligned(16))) u32 s_qb[64];
    __shared__ __attribute__((aligned(16))) float s_o[16 * 132];
    __shared__ __attribute__((aligned(16))) f32x2 s_ml[16];
    __shared__ __attribute__((aligned(16))) float s_r[64 * 20];
    __shared__ __attribute__((aligned(16))) float s_r2[64 * 20];
    __shared__ __attribute__((aligned(16))) u16 s_hn[4 * 520];
    __shared__ __attribute__((aligned(16))) float s_gu[8 * GU_BLK];
    __shared__ __attribute__((aligned(16))) u32 s_go[4];

    const int t = threadIdx.x, l = t & 63, g = l >> 4, n = l & 15;
    const int w = __builtin_amdgcn_readfirstlane(t >> 6);
    const int wg = blockIdx.x;
    const int h = (wg & 7) * 4 + (wg >> 6), jj = (wg >> 3) & 7, b = jj >> 1, half = jj & 1;
    const int bh = b * H + h, gi = h * 8 + jj;
    const bool is_red = wg < NRED, is_mlp = wg >= NRED && wg < NRED + NMLP;
    const int mx7 = wg & 7;
    const int mj = mx7 * 5 + (mx7 < 3 ? mx7 : 3) + ((wg - NRED) >> 3);

    const rsrc_t r_ws = mk_rsrc(ws, WS_USED);

    const size_t kvbase = ((size_t)bh * SK + (SK - W) + half * 512) * DH;
    const u32 kvo = w * 8192 + l * 16;
    const rsrc_t r_k = mk_rsrc(kc + kvbase, 512 * DH * 2);
    const rsrc_t r_v = mk_rsrc(vc + kvbase, 512 * DH * 2);
    u32x4 kv[8], vv[8];
    u32 tg0 = 0, tg1 = 0;
    u32 pv = 0;

    if (w == 0) {
        __builtin_amdgcn_s_setprio(3);
        s_qb[l] = SENT;
        if (l < 4) s_go[l] = 0;
        const u32x2 ev = ld8_sc1(r_ws, OFF_E);
        pin();
        {
            const u32x4 s4 = {SENT, SENT, SENT, SENT};
            st16_sc1(s4, r_ws, OFF_Q + gi * 2048 + l * 16);
            st16_sc1(s4, r_ws, OFF_Q + gi * 2048 + 1024 + l * 16);
            if (l < 33) st16_sc1(s4, r_ws, OFF_O + gi * O_STRIDE + l * 16);
            st16_sc1(s4, r_ws, OFF_Y + gi * 2048 + l * 16);
            st16_sc1(s4, r_ws, OFF_Y + gi * 2048 + 1024 + l * 16);
            if (is_red && l < 8) st16_sc1(s4, r_ws, OFF_H + wg * 128 + l * 16);
            if (is_mlp) {
#pragma unroll
                for (int i = 0; i < 8; ++i) st16_sc1(s4, r_ws, OFF_D + mj * 8192 + i * 1024 + l * 16);
            }
        }
        asm volatile("" ::: "memory");
        pin();
        const rsrc_t r_x = mk_rsrc(x, NB * D * 2);
        u32x4 xs[4], xf[2], wv[16];
#pragma unroll
        for (int i = 0; i < 4; ++i) xs[i] = ldx<X_AUX>(r_x, (n >> 2) * 1024 + ((n & 3) * 4 + g) * 64 + i * 16, 0);
#pragma unroll
        for (int s = 0; s < 2; ++s) xf[s] = ldx<X_AUX>(r_x, (n >> 2) * 1024 + jj * 128 + s * 64 + g * 16, 0);
        {
            const rsrc_t r = mk_rsrc(wq + h * DH, (D * H * DH - h * DH) * 2);
            const u32 o = (u32)(jj * 64 + g * 8) * 8192u + n * 16;
#pragma unroll
            for (int i = 0; i < 16; ++i) wv[i] = ldx<WQ_AUX>(r, o, ((i >> 3) * 32 + (i & 7)) * 8192);
        }
        pin();
        asm volatile("s_waitcnt vmcnt(22)" ::: "memory");
        pin();
        {
            const u64 e = ((u64)(u32)__builtin_amdgcn_readfirstlane(ev[1]) << 32) | (u32)__builtin_amdgcn_readfirstlane(ev[0]);
            const u64 tag = e * 6364136223846793005ull + 1442695040888963407ull;
            tg0 = (u32)tag; tg1 = (u32)(tag >> 32);
        }
        if (l == 0) st8_sc1(u32x2{tg0, tg1}, r_ws, OFF_FLAG + gi * 8);
        pin();
        lds_barrier();
        pin();
        float f[32];
#pragma unroll
        for (int i = 0; i < 4; ++i) unpack8(xs[i], f + 8 * i);
        float s0 = 0.f, s1 = 0.f, s2 = 0.f, s3 = 0.f;
#pragma unroll
        for (int i = 0; i < 8; ++i) { s0 += f[i]; s1 += f[8 + i]; s2 += f[16 + i]; s3 += f[24 + i]; }
        float sm = (s0 + s1) + (s2 + s3);
        sm += dpp<DPP_X1>(sm); sm += dpp<DPP_X2>(sm);
        const float mean = xrow_sum(sm) * (1.f / D);
        s0 = s1 = s2 = s3 = 0.f;
#pragma unroll
        for (int i = 0; i < 8; ++i) {
            const float d0 = f[i] - mean, d1 = f[8 + i] - mean, d2 = f[16 + i] - mean, d3 = f[24 + i] - mean;
            s0 += d0 * d0; s1 += d1 * d1; s2 += d2 * d2; s3 += d3 * d3;
        }
        float sq = (s0 + s1) + (s2 + s3);
        sq += dpp<DPP_X1>(sq); sq += dpp<DPP_X2>(sq);
        const float var = xrow_sum(sq) * (1.f / D);
        const float mb = bfr(mean);
        const float rs = __builtin_amdgcn_rcpf(bfr(__builtin_amdgcn_sqrtf(bfr(bfr(var) + 1e-5f))));
        bf16x8 xa0 = asfrag(ln_apply(xf[0], mb, rs)), xa1 = asfrag(ln_apply(xf[1], mb, rs));
        keep(xa0); keep(xa1);
        pin();
        u32x4 fl = ld16_sc1(r_ws, OFF_FLAG + h * 64 + (l & 3) * 16);
        pin();
        float qp[8];
#pragma unroll
        for (int c = 0; c < 8; ++c) {
            f32x4 a = mfma(xa0, colfrag(wv, c), f32x4{0.f, 0.f, 0.f, 0.f});
            qp[c] = mfma(xa1, colfrag(wv + 8, c), a)[0];
        }
        const u32 qo = OFF_Q + gi * 2048 + l * 16;
        st16_sc1(canon4(f32x4{qp[0], qp[1], qp[2], qp[3]}), r_ws, qo);
        st16_sc1(canon4(f32x4{qp[4], qp[5], qp[6], qp[7]}), r_ws, qo + 1024);
        pin();
        asm volatile("s_barrier" ::: "memory");
        pin();
        _Pragma("nounroll") for (u32 spin = 0; spin < SPIN_CAP; ++spin) {
            if (__all(((fl[0] ^ tg0) | (fl[1] ^ tg1) | (fl[2] ^ tg0) | (fl[3] ^ tg1)) == 0)) break;
            asm volatile("" ::: "memory");
            fl = ld16_sc1(r_ws, OFF_FLAG + h * 64 + (l & 3) * 16);
        }
        asm volatile("" ::: "memory");
        {
            const u32 o = OFF_Q + (u32)(h * 8 + (g >> 1)) * 2048u + (g & 1) * 1024 + (b * 16 + n) * 16;
            u32x4 qv[4];
            _Pragma("nounroll") for (u32 spin = 0; spin < SPIN_CAP; ++spin) {
                asm volatile("" ::: "memory");
#pragma unroll
                for (int i = 0; i < 4; ++i) qv[i] = ld16_sc1(r_ws, o + i * 4096);
                if (__all(umax(umax(umax4(qv[0]), umax4(qv[1])), umax(umax4(qv[2]), umax4(qv[3]))) != SENT)) break;
            }
            float q4[4];
#pragma unroll
            for (int j = 0; j < 4; ++j) {
                const float v = (__uint_as_float(qv[0][j]) + __uint_as_float(qv[1][j])) + (__uint_as_float(qv[2][j]) + __uint_as_float(qv[3][j]));
                auto sw = __builtin_amdgcn_permlane32_swap(__float_as_uint(v), __float_as_uint(v), false, false);
                q4[j] = __uint_as_float(sw[0]) + __uint_as_float(sw[1]);
            }
            if (l < 32) *(u32x2*)&s_qb[n * 4 + g * 2] = u32x2{canon(pk_bf16(q4[0], q4[1])), canon(pk_bf16(q4[2], q4[3]))};
        }
        pin();
#pragma unroll
        for (int i = 0; i < 8; ++i) kv[i] = ldx<K_AUX>(r_k, kvo + (i & 3) * 1024, (i >> 2) * 4096);
    } else {
#pragma unroll
        for (int i = 0; i < KPRE; ++i) kv[i] = ldx<K_AUX>(r_k, kvo + (i & 3) * 1024, (i >> 2) * 4096);
        pin();
        asm volatile("s_barrier" ::: "memory");
        pin();
#pragma unroll
        for (int i = KPRE; i < 8; ++i) kv[i] = ldx<K_AUX>(r_k, kvo + (i & 3) * 1024, (i >> 2) * 4096);
        pin();
        asm volatile("s_barrier" ::: "memory");
    }
    pin();
#pragma unroll
    for (int i = 0; i < 8; ++i) vv[i] = ldx<V_AUX>(r_v, kvo + (i & 3) * 1024, (i >> 2) * 4096);
    u32x4 fl0 = {0, 0, 0, 0}, fl1 = {0, 0, 0, 0};
    const bool chk = (w == 0) && (is_red || is_mlp);
    if (chk) { fl0 = ld16_sc1(r_ws, OFF_FLAG + l * 16); fl1 = ld16_sc1(r_ws, OFF_FLAG + 1024 + l * 16); }
    pin();
    if (w == 0) s_go[l & 3] = 1;
    pin();

    u32x4 qf;
    _Pragma("nounroll") for (u32 spin = 0; spin < SPIN_CAP; ++spin) {
        qf = lds_poll16(&s_qb[n * 4]);
        if (__all(full4(qf))) break;
        __builtin_amdgcn_s_sleep(2);
    }

    float sc[8];
#pragma unroll
    for (int i = 0; i < 8; ++i) {
        float a = 0.f;
#pragma unroll
        for (int j = 0; j < 4; ++j)
            a = dot2(kv[i][j], qf[j], a);
        sc[i] = row16_sum(a);
    }
#pragma unroll
    for (int i = 0; i < 8; i += 2) {
        u32 p = pk_bf16(sc[i], sc[i + 1]);
        p = pk_bf16(bf_lo(p) * 0.08838834764831845f, bf_hi(p) * 0.08838834764831845f);
        sc[i] = bf_lo(p); sc[i + 1] = bf_hi(p);
    }
    float mx = fmaxf(fmaxf(fmaxf(sc[0], sc[1]), fmaxf(sc[2], sc[3])), fmaxf(fmaxf(sc[4], sc[5]), fmaxf(sc[6], sc[7])));
    mx = xrow_max(mx);
    u32x4 pa;
    float ls = 0.f;
#pragma unroll
    for (int i = 0; i < 8; i += 2) {
        const float p0 = __builtin_amdgcn_exp2f((sc[i] - mx) * 1.4426950408889634f);
        const float p1 = __builtin_amdgcn_exp2f((sc[i + 1] - mx) * 1.4426950408889634f);
        ls += p0 + p1;
        pa[i >> 1] = pk_bf16(p0, p1);
    }
    ls = xrow_sum(ls);
    if (l == 0) s_ml[w] = f32x2{mx, ls};
    pin();

    {
        float op[8];
#pragma unroll
        for (int c = 0; c < 8; ++c) op[c] = mfma(asfrag(pa), colfrag(vv, c), f32x4{0.f, 0.f, 0.f, 0.f})[0];
        if (l < 16) {
            *(f32x4*)&s_o[w * 132 + n * 8] = f32x4{op[0], op[1], op[2], op[3]};
            *(f32x4*)&s_o[w * 132 + n * 8 + 4] = f32x4{op[4], op[5], op[6], op[7]};
        }
    }
    pin();
    const int kh = jj >> 2, cq = jj & 3;
    u32x4 wb[16];
    const rsrc_t r_wo = mk_rsrc(wo + (size_t)(h * DH + kh * 64) * D + cq * 128, (64 * D - cq * 128) * 2);
    const u32 woo = (u32)g * 8192u + n * 16;
    if (w == 1) {
        _Pragma("nounroll") for (u32 spin = 0; spin < SPIN_CAP; ++spin) {
            if (lds_poll16(s_go)[0]) break;
            __builtin_amdgcn_s_sleep(4);
        }
#pragma unroll
        for (int i = 0; i < 16; ++i) wb[i] = ldx<WO_AUX>(r_wo, woo + (i & 3) * 1024, (i >> 2) * 4096 + (i >> 3) * 24576);
    }
    if (chk) {
        _Pragma("nounroll") for (u32 spin = 0; spin < SPIN_CAP; ++spin) {
            const bool ok = ((fl0[0] ^ tg0) | (fl0[1] ^ tg1) | (fl0[2] ^ tg0) | (fl0[3] ^ tg1) |
                             (fl1[0] ^ tg0) | (fl1[1] ^ tg1) | (fl1[2] ^ tg0) | (fl1[3] ^ tg1)) == 0;
            if (__all(ok)) break;
            asm volatile("" ::: "memory");
            fl0 = ld16_sc1(r_ws, OFF_FLAG + l * 16); fl1 = ld16_sc1(r_ws, OFF_FLAG + 1024 + l * 16);
        }
        if (wg == 0 && l == 0) st8_sc1(u32x2{tg0, tg1}, r_ws, OFF_E);
    }
    pin();
    lds_barrier();

    if (w >= 4 && w < 8) {
        const int u = w - 4;
        const f32x2 ml = s_ml[n];
        const f32x4 a = *(const f32x4*)&s_o[n * 132 + u * 32 + g * 8], a2 = *(const f32x4*)&s_o[n * 132 + u * 32 + g * 8 + 4];
        const float M = row16_max(ml[0]);
        const float f = __builtin_amdgcn_exp2f((ml[0] - M) * 1.4426950408889634f);
        f32x4 v, v2;
#pragma unroll
        for (int i = 0; i < 4; ++i) { v[i] = row16_sum(a[i] * f); v2[i] = row16_sum(a2[i] * f); }
        const float L = row16_sum(ml[1] * f);
        if (n < 2) st16_sc1(canon4(n ? v2 : v), r_ws, OFF_O + gi * O_STRIDE + (u * 32 + g * 8 + n * 4) * 4);
        if (u == 0 && l == 0) st8_sc1(u32x2{canon(__float_as_uint(M)), canon(__float_as_uint(L))}, r_ws, OFF_O + gi * O_STRIDE + 512);
    } else if (w == 1) {
        __builtin_amdgcn_s_setprio(3);
        const u32 so = OFF_O + (u32)(h * 8 + (n >> 2) * 2 + (n & 1)) * O_STRIDE;
        const u32 oo = so + (kh * 64 + ((n >> 1) & 1) * 32 + g * 8) * 4;
        u32x4 o0, o1;
        u32x2 ml;
        bf16x8 b0[8], b1[8];
#pragma unroll
        for (int c = 0; c < 8; ++c) { b0[c] = colfrag(wb, c); keep(b0[c]); if (c & 1) pin(); }
#pragma unroll
        for (int c = 0; c < 8; ++c) { b1[c] = colfrag(wb + 8, c); keep(b1[c]); if (c & 1) pin(); }
        pin();
        _Pragma("nounroll") for (u32 spin = 0; spin < SPIN_CAP; ++spin) {
            asm volatile("" ::: "memory");
            o0 = ld16_sc1(r_ws, oo); o1 = ld16_sc1(r_ws, oo + 16); ml = ld8_sc1(r_ws, so + 512);
            if (__all(umax(umax(umax4(o0), umax4(o1)), umax(ml[0], ml[1])) != SENT)) break;
        }
        const float m = __uint_as_float(ml[0]);
        const float M = fmaxf(m, dpp<DPP_X1>(m));
        const float e = __builtin_amdgcn_exp2f((m - M) * 1.4426950408889634f);
        const float le = __uint_as_float(ml[1]) * e;
        const float wt = e * __builtin_amdgcn_rcpf(le + dpp<DPP_X1>(le));
        float v[8];
#pragma unroll
        for (int i = 0; i < 4; ++i) { v[i] = __uint_as_float(o0[i]) * wt; v[4 + i] = __uint_as_float(o1[i]) * wt; }
#pragma unroll
        for (int i = 0; i < 8; ++i) v[i] += dpp<DPP_X1>(v[i]);
        const bf16x8 oa = asfrag(u32x4{pk_bf16(v[0], v[1]), pk_bf16(v[2], v[3]), pk_bf16(v[4], v[5]), pk_bf16(v[6], v[7])});
        float yp[8];
#pragma unroll
        for (int c = 0; c < 8; ++c)
            yp[c] = mfma(oa, b0[c], f32x4{0.f, 0.f, 0.f, 0.f})[0] + mfma(oa, b1[c], f32x4{0.f, 0.f, 0.f, 0.f})[2];
        const u32 yo = OFF_Y + gi * 2048 + l * 16;
        st16_sc1(canon4(f32x4{yp[0], yp[1], yp[2], yp[3]}), r_ws, yo);
        st16_sc1(canon4(f32x4{yp[4], yp[5], yp[6], yp[7]}), r_ws, yo + 1024);
    }
    lds_barrier();
    pin();
    if (wg >= 80 && w < 8) {
        const int m0 = mx7 * 5 + (mx7 < 3 ? mx7 : 3), nm = mx7 < 3 ? 6 : 5;
        const int li = ((wg >> 3) - 10) * 8 + w;
        if (li < 128) {
            const int seg = (l & 7) < nm ? (l & 7) : nm - 1, row = (li >> 1) * 8 + (l >> 3);
            const rsrc_t r = mk_rsrc((li & 1) ? wu : wg_, D * F * 2);
            pv = __builtin_amdgcn_raw_buffer_load_b32(r, (u32)row * (F * 2) + (m0 + seg) * 64, 0, PF_AUX);
        } else {
            const int r0 = (li - 128) * 4 + g, row = r0 < nm * 32 ? r0 : nm * 32 - 1;
            const rsrc_t r = mk_rsrc(wd, F * D * 2);
            pv = __builtin_amdgcn_raw_buffer_load_b32(r, (u32)(m0 * 32 + row) * 1024u + n * 64, 0, PF_AUX);
        }
    }

    if (is_red) {
        const int rb = wg >> 3, ch = wg & 7;
        float xres = 0.f;
        if (w == 0) xres = __uint_as_float((u32)x[rb * D + ch * 64 + l] << 16);
        const int p = w * 4 + g;
        const u32 sub = (n >> 3) * 1024 + rb * 256 + (ch & 1) * 128 + (n & 7) * 16;
        const u32 yo = OFF_Y + (u32)((p >> 1) * 8 + (p & 1) * 4 + (ch >> 1)) * 2048u + sub;
        u32x4 yv;
        _Pragma("nounroll") for (u32 spin = 0; spin < SPIN_CAP; ++spin) {
            asm volatile("" ::: "memory");
            yv = ld16_sc1(r_ws, yo);
            if (__all(full4(yv))) break;
        }
        {
            float v[4];
#pragma unroll
            for (int i = 0; i < 4; ++i) v[i] = xrow_sum(__uint_as_float(yv[i]));
            if (l < 16) {
#pragma unroll
                for (int i = 0; i < 4; ++i) s_r[((n & 7) * 8 + (n >> 3) * 4 + i) * 20 + w] = v[i];
            }
        }
        lds_barrier();
        float hval = 0.f;
        if (w == 0) {
            const f32x4 a = *(const f32x4*)&s_r[l * 20], bb = *(const f32x4*)&s_r[l * 20 + 4];
            const f32x4 c = *(const f32x4*)&s_r[l * 20 + 8], d = *(const f32x4*)&s_r[l * 20 + 12];
            const f32x4 e = (a + bb) + (c + d);
            hval = bfr(xres + bfr((e[0] + e[1]) + (e[2] + e[3])));
            const u32 pr = __builtin_amdgcn_perm(dppu<DPP_SHL1>(__float_as_uint(hval)), __float_as_uint(hval), 0x07060302u);
            const u32x4 hv = {canon(pr), canon(dppu<DPP_SHL2>(pr)), canon(dppu<DPP_SHL4>(pr)), canon(dppu<DPP_SHL6>(pr))};
            if ((l & 7) == 0) st16_sc1(hv, r_ws, OFF_H + wg * 128 + l * 2);
        }
        if (w < 11) {
            const int pj = w * 4 + g;
            const int pjc = pj < NMLP ? pj : NMLP - 1;
            const u32 o = OFF_D + (u32)pjc * 8192u + (ch >> 1) * 2048 + sub;
            u32x4 dv;
            _Pragma("nounroll") for (u32 spin = 0; spin < SPIN_CAP; ++spin) {
                asm volatile("" ::: "memory");
                dv = ld16_sc1(r_ws, o);
                if (__all(full4(dv))) break;
            }
            float v[4];
#pragma unroll
            for (int i = 0; i < 4; ++i) v[i] = xrow_sum(pj < NMLP ? __uint_as_float(dv[i]) : 0.f);
            if (l < 16) {
#pragma unroll
                for (int i = 0; i < 4; ++i) s_r2[((n & 7) * 8 + (n >> 3) * 4 + i) * 20 + w] = v[i];
            }
        }
        lds_barrier();
        if (w == 0) {
            const f32x4 a = *(const f32x4*)&s_r2[l * 20], bb = *(const f32x4*)&s_r2[l * 20 + 4], c = *(const f32x4*)&s_r2[l * 20 + 8];
            const f32x4 e = a + bb;
            const float sum = ((e[0] + e[1]) + (e[2] + e[3])) + ((c[0] + c[1]) + c[2]);
            const float o = hval + bfr(sum);
            out[rb * D + ch * 64 + l] = (u16)pk_bf16(o, o);
        }
    } else if (is_mlp) {
        if (w >= 8) {
            const int mat = w & 1, kq = (w - 8) >> 1, kb = n >> 2, chunk = n & 3;
            const rsrc_t r = mk_rsrc(mat ? wu : wg_, D * F * 2);
            const u32 o = (u32)(kq * 128 + kb * 32 + g * 8) * (F * 2) + mj * 64 + chunk * 16;
            u32x4 w0[8];
#pragma unroll
            for (int i = 0; i < 8; ++i) w0[i] = ldx<MW_AUX>(r, o, i * (F * 2));
            bf16x8 f0[8];
#pragma unroll
            for (int c = 0; c < 8; ++c) { f0[c] = colfrag(w0, c); keep(f0[c]); }
            pin();
            lds_barrier();
            const bf16x8 a0 = asfrag(*(const u32x4*)&s_hn[(n >> 2) * 520 + kq * 128 + (n & 3) * 32 + g * 8]);
            f32x4 acc[8];
#pragma unroll
            for (int c = 0; c < 8; ++c) acc[c] = mfma(a0, f0[c], f32x4{0.f, 0.f, 0.f, 0.f});
            float lo = kb_sum<0>(acc[0]), hi = kb_sum<0>(acc[4]);
            lo = bank_put<1>(lo, kb_sum<1>(acc[1])); hi = bank_put<1>(hi, kb_sum<1>(acc[5]));
            lo = bank_put<2>(lo, kb_sum<2>(acc[2])); hi = bank_put<2>(hi, kb_sum<2>(acc[6]));
            lo = bank_put<3>(lo, kb_sum<3>(acc[3])); hi = bank_put<3>(hi, kb_sum<3>(acc[7]));
            *(f32x2*)&s_gu[(mat * 4 + kq) * GU_BLK + chunk * GU_CH + g * 8 + kb * 2] = f32x2{lo, hi};
            lds_barrier();
        } else if (w >= 4) {
            const int c4 = w - 4;
            const rsrc_t r = mk_rsrc(wd + (size_t)mj * 32 * D, 32 * D * 2);
            const u32 o = (u32)(g * 8) * 1024u + c4 * 256 + n * 16;
            u32x4 w0[8];
#pragma unroll
            for (int i = 0; i < 8; ++i) w0[i] = ldx<MW_AUX>(r, o + (i >> 1) * 1024, (i & 1) * 4096);
            bf16x8 f0[8];
#pragma unroll
            for (int c = 0; c < 8; ++c) { f0[c] = colfrag(w0, c); keep(f0[c]); }
            pin();
            lds_barrier();
            lds_barrier();
            const float* gp = &s_gu[g * GU_CH + n * 2];
            f32x2 pg[4], pu[4];
#pragma unroll
            for (int i = 0; i < 4; ++i) { pg[i] = *(const f32x2*)(gp + i * GU_BLK); pu[i] = *(const f32x2*)(gp + (4 + i) * GU_BLK); }
            const f32x2 sg = (pg[0] + pg[1]) + (pg[2] + pg[3]), su = (pu[0] + pu[1]) + (pu[2] + pu[3]);
            const u32 gb = pk_bf16(sg[0], sg[1]), ub = pk_bf16(su[0], su[1]);
            const u32 ap = pk_bf16(bf_lo(gb) * bf_lo(ub), bf_hi(gb) * bf_hi(ub));
            const bf16x8 aa = asfrag(u32x4{dppu<0x00>(ap), dppu<0x55>(ap), dppu<0xAA>(ap), dppu<0xFF>(ap)});
            float dp[8];
#pragma unroll
            for (int c = 0; c < 8; ++c) dp[c] = mfma(aa, f0[c], f32x4{0.f, 0.f, 0.f, 0.f})[0];
            const u32 so = OFF_D + (u32)mj * 8192u + c4 * 2048 + l * 16;
            st16_sc1(canon4(f32x4{dp[0], dp[1], dp[2], dp[3]}), r_ws, so);
            st16_sc1(canon4(f32x4{dp[4], dp[5], dp[6], dp[7]}), r_ws, so + 1024);
        } else {
            const int hb = w;
            u32x4 hv;
            _Pragma("nounroll") for (u32 spin = 0; spin < SPIN_CAP; ++spin) {
                asm volatile("" ::: "memory");
                hv = ld16_sc1(r_ws, OFF_H + hb * 1024 + l * 16);
                if (__all(full4(hv))) break;
            }
            float mb, rs;
            ln_stats(hv, mb, rs);
            *(u32x4*)&s_hn[hb * 520 + l * 8] = ln_apply(hv, mb, rs);
            lds_barrier();
        }
    }
    asm volatile("" :: "v"(pv));
}

extern "C" void kernel_launch(void* const* d_in, const int* in_sizes, int n_in, void* d_out, int out_size, void* d_ws,
                              size_t ws_size, hipStream_t stream) {
    fused_block<<<NWG, 1024, 0, stream>>>((const u16*)d_in[0], (const u16*)d_in[1], (const u16*)d_in[2], (const u16*)d_in[3],
                                          (const u16*)d_in[4], (const u16*)d_in[6], (const u16*)d_in[7], (const u16*)d_in[8],
                                          (u16*)d_out, (char*)d_ws);
}
```
